```python
import torch

def _softmax(x, axis=-1):
    return torch.softmax(x, dim=axis)

def _gelu(x):
    return torch.nn.functional.gelu(x, approximate='tanh')

def _silu(x):
    return torch.nn.functional.silu(x)

def _sigmoid(x):
    return torch.sigmoid(x)

def _layernorm(x, axis=-1, eps=1e-05):
    m = x.mean(dim=axis, keepdim=True)
    v = x.var(dim=axis, keepdim=True, unbiased=False)
    return (x - m) / torch.sqrt(v + eps)

def _scatter_add(tgt, idx, src, axis=0):
    return tgt.clone().index_add_(axis, idx, src)

def _segment_sum(src, seg, n):
    out = torch.zeros((n,) + src.shape[1:], dtype=src.dtype)
    return out.index_add_(0, seg, src)

def _pad(x, pw):
    flat = tuple((v for pair in reversed(pw) for v in pair))
    return torch.nn.functional.pad(x, flat)
kernel_name = 'sequential_preLN__spectral_gfnet__mixer_gmlp__prompt'

def setup_inputs():

    _w = lambda a: (lambda t: t.to(torch.bfloat16) if t.is_floating_point() else t)(torch.as_tensor(a))

    def _A_setup():
        B, S, D = (1, 2048, 640)
        Df = D // 2 + 1
        return {'x': _w((lambda *s: torch.randn(*s, dtype=torch.bfloat16) * 0.05)(B, S, D)), 'w_r': _w((lambda *s: torch.randn(*s, dtype=torch.bfloat16) * 0.4)(S, Df) * 0.02), 'w_i': _w((lambda *s: torch.randn(*s, dtype=torch.bfloat16) * 0.4)(S, Df) * 0.02)}
    _A_in = _A_setup()

    def _B_setup():
        B, S, D, F = (1, 2048, 640, 1728)
        Fh = F // 2
        return {'x': _w((lambda *s: torch.randn(*s, dtype=torch.bfloat16) * 0.05)(B, S, D)), 'w_in': _w((lambda *s: torch.randn(*s, dtype=torch.bfloat16) * 0.4)(D, 2 * Fh)), 'w_sp': _w(0.01 * (lambda *s: torch.randn(*s, dtype=torch.bfloat16) * 0.4)(S, S)), 'b_sp': _w(1.0 + 0.01 * (lambda *s: torch.randn(*s, dtype=torch.bfloat16) * 0.05)(S)), 'w_out': _w((lambda *s: torch.randn(*s, dtype=torch.bfloat16) * 0.05)(Fh, D))}
    _B_in = _B_setup()
    return {'A_x': _A_in['x'], 'A_w_r': _A_in['w_r'], 'A_w_i': _A_in['w_i'], 'B_x': _B_in['x'], 'B_w_in': _B_in['w_in'], 'B_w_sp': _B_in['w_sp'], 'B_b_sp': _B_in['b_sp'], 'B_w_out': _B_in['w_out']}

def reference(A_x, A_w_r, A_w_i, B_x, B_w_in, B_w_sp, B_b_sp, B_w_out):

    def _A_ref(x, w_r, w_i):
        B, S, D = x.shape
        X = (lambda x, axes=(-2, -1): torch.fft.rfft2(x.float(), dim=axes))(x, axes=(-2, -1))
        W = w_r + 1j * w_i
        X = X * W
        y = (lambda x, s, axes=(-2, -1): torch.fft.irfft2(x, s=s, dim=axes).to(torch.bfloat16))(X, (S, D), axes=(-2, -1))
        return y

    def _B_ref(x, w_in, w_sp, b_sp, w_out):
        B, S, D = x.shape
        F = w_in.shape[-1]
        Fh = F // 2
        z = _gelu(torch.einsum('bsd,df->bsf', x, w_in))
        z1 = z[..., :Fh].clone()
        z2 = _layernorm(z[..., Fh:], axis=-1)
        g = torch.einsum('ts,bsf->btf', w_sp, z2) + b_sp[None, :, None]
        return torch.einsum('bsf,fd->bsd', z1 * g, w_out)
    x = A_x
    h = x + _A_ref(_layernorm(x, axis=-1), A_w_r, A_w_i)
    h2 = h + _B_ref(_layernorm(h, axis=-1), B_w_in, B_w_sp, B_b_sp, B_w_out)
    return h2
```

```cpp
#include <hip/hip_runtime.h>
#include <stdint.h>

typedef unsigned int u32;
typedef unsigned short u16;
typedef unsigned long long u64;
typedef _Float16 h2 __attribute__((ext_vector_type(2)));
typedef _Float16 h4 __attribute__((ext_vector_type(4)));
typedef _Float16 h8 __attribute__((ext_vector_type(8)));
typedef short s4 __attribute__((ext_vector_type(4)));
typedef __bf16 b2 __attribute__((ext_vector_type(2)));
typedef __bf16 b8 __attribute__((ext_vector_type(8)));
typedef float f2 __attribute__((ext_vector_type(2)));
typedef float f4 __attribute__((ext_vector_type(4)));
typedef u32 u32x2 __attribute__((ext_vector_type(2)));
typedef u32 u32x4 __attribute__((ext_vector_type(4)));
#define DI __device__ __forceinline__
#define LDS_AS __attribute__((address_space(3)))
#define GLB_AS __attribute__((address_space(1)))

constexpr int S = 2048, D = 640, FH = 864, F = 1728, KT = 321;

constexpr size_t MB = 1u << 20;
constexpr size_t WS_T1 = 0 * MB;
constexpr size_t WS_T2 = 3 * MB;
constexpr size_t WS_WT = 6 * MB;
constexpr size_t WS_H = 9 * MB;
constexpr size_t WS_HN = 12 * MB;
constexpr size_t WS_WIN = 15 * MB;
constexpr size_t WS_WOUT = 18 * MB;
constexpr size_t WS_Z1 = 20 * MB;
constexpr size_t WS_Z2 = 24 * MB;
constexpr size_t WS_P = 28 * MB;
constexpr size_t WS_STAT = 32 * MB;
constexpr size_t WS_WMAX = 33 * MB;

constexpr double kPi = 3.14159265358979323846264338327950288;
constexpr double c_sin_small(double x) {
  double t = x, s = x;
  for (int i = 1; i < 10; ++i) { t *= -x * x / ((2 * i) * (2 * i + 1)); s += t; }
  return s;
}
constexpr double c_cos_small(double x) {
  double t = 1, s = 1;
  for (int i = 1; i < 10; ++i) { t *= -x * x / ((2 * i - 1) * (2 * i)); s += t; }
  return s;
}
struct Cos640 { double c[640]; };
constexpr Cos640 make_cos640() {
  Cos640 t{};
  for (int j = 0; j <= 160; ++j) {
    double v = (j <= 80) ? c_cos_small(2 * kPi * j / 640) : c_sin_small(2 * kPi * (160 - j) / 640);
    if (j == 160) v = 0.0;
    t.c[j] = v;
    t.c[(640 - j) % 640] = v;
    t.c[320 - j] = -v;
    t.c[(320 + j) % 640] = -v;
  }
  return t;
}
constexpr Cos640 kCos = make_cos640();
constexpr double cs(int j) { return kCos.c[((j % 640) + 640) % 640]; }
constexpr double sn(int j) { return kCos.c[(((j - 160) % 640) + 640) % 640]; }
constexpr u32 pkh(double lo, double hi) {
  return (u32)__builtin_bit_cast(u16, (_Float16)(float)lo) | ((u32)__builtin_bit_cast(u16, (_Float16)(float)hi) << 16);
}
constexpr u32 fb(double v) { return __builtin_bit_cast(u32, (float)v); }
constexpr double kRs40 = 0.15811388300841896659994467722163593;

struct Tab { u32 v[12][64][4]; };
constexpr Tab make_tab_fwd() {
  Tab t{};
  for (int l = 0; l < 64; ++l) {
    int n = l & 15, g = l >> 4;
    for (int nt = 0; nt < 2; ++nt) {
      int k1 = 16 * nt + n;
      bool ok = k1 <= 20;
      for (int q = 0; q < 4; ++q) {
        int a0 = (q < 2) ? 4 * g + 2 * q : 16 + 4 * g + 2 * (q - 2), a1 = a0 + 1;
        t.v[0 + nt][l][q] = ok ? pkh(cs(16 * a0 * k1), cs(16 * a1 * k1)) : 0u;
        t.v[2 + nt][l][q] = ok ? pkh(-sn(16 * a0 * k1), -sn(16 * a1 * k1)) : 0u;
      }
      for (int q = 0; q < 2; ++q) {
        int a0 = 32 + 4 * g + 2 * q, a1 = a0 + 1;
        bool v = ok && a0 < 40;
        t.v[4][l][2 * nt + q] = v ? pkh(cs(16 * a0 * k1), cs(16 * a1 * k1)) : 0u;
        t.v[5][l][2 * nt + q] = v ? pkh(-sn(16 * a0 * k1), -sn(16 * a1 * k1)) : 0u;
      }
      for (int r = 0; r < 4; ++r) {
        int b = 4 * g + r;
        t.v[6 + 2 * nt][l][r] = fb(cs(b * k1) * kRs40);
        t.v[7 + 2 * nt][l][r] = fb(-sn(b * k1) * kRs40);
      }
    }
    for (int q = 0; q < 4; ++q) {
      int b = 4 * g + q, k2 = n;
      double c = cs(40 * b * k2) * 0.25, s = sn(40 * b * k2) * 0.25, sg = (k2 >= 8) ? -1.0 : 1.0;
      t.v[10][l][q] = pkh(c, s);
      t.v[11][l][q] = pkh(-s * sg, c * sg);
    }
  }
  return t;
}
constexpr Tab make_tab_inv() {
  Tab t{};
  for (int l = 0; l < 64; ++l) {
    int n = l & 15, g = l >> 4;
    for (int q = 0; q < 4; ++q) {
      int k2 = 4 * g + q, b = n;
      double c = cs(40 * b * k2) * 0.25, s = sn(40 * b * k2) * 0.25, sg = (k2 >= 8) ? -1.0 : 1.0;
      t.v[0][l][q] = pkh(c, -s * sg);
      t.v[1][l][q] = pkh(s, c * sg);
    }
    for (int mt = 0; mt < 2; ++mt) {
      for (int r = 0; r < 4; ++r) {
        int k1 = 16 * mt + 4 * g + r, b = n;
        t.v[2 + 2 * mt][l][r] = fb(cs(b * k1) * kRs40);
        t.v[3 + 2 * mt][l][r] = fb(sn(b * k1) * kRs40);
      }
      for (int nt = 0; nt < 3; ++nt) {
        int a = 16 * nt + n;
        for (int q = 0; q < 4; ++q) {
          int k1 = 16 * mt + 4 * g + q;
          double w = (k1 == 0 || k1 == 20) ? 1.0 : 2.0;
          bool ok = k1 <= 20 && a < 40;
          t.v[6 + 2 * nt + mt][l][q] = ok ? pkh(w * cs(16 * a * k1), -w * sn(16 * a * k1)) : 0u;
        }
      }
    }
  }
  return t;
}
static __device__ constexpr Tab g_tab_fwd = make_tab_fwd();
static __device__ constexpr Tab g_tab_inv = make_tab_inv();

DI float bf_lo(u32 v) { return __builtin_bit_cast(float, v << 16); }
DI float bf_hi(u32 v) { return __builtin_bit_cast(float, v & 0xffff0000u); }
DI u32 pk_bf(float a, float b) { f2 x = {a, b}; return __builtin_bit_cast(u32, __builtin_convertvector(x, b2)); }
DI u32 pk_h(float a, float b) { f2 x = {a, b}; return __builtin_bit_cast(u32, __builtin_convertvector(x, h2)); }
DI float rbf(float a) { return bf_lo(pk_bf(a, a)); }
template <int CTRL> DI float dpp(float v) {
  return __builtin_bit_cast(float, __builtin_amdgcn_update_dpp(0, __builtin_bit_cast(int, v), CTRL, 0xf, 0xf, false));
}
DI float row16_sum(float v) {
  v += dpp<0x128>(v); v += dpp<0x124>(v); v += dpp<0x122>(v); v += dpp<0x121>(v);
  return v;
}
DI float wave_sum(float v) {
  v = row16_sum(v);
  int x = __builtin_bit_cast(int, v);
  float a = __builtin_bit_cast(float, __builtin_amdgcn_readlane(x, 0));
  float b = __builtin_bit_cast(float, __builtin_amdgcn_readlane(x, 16));
  float c = __builtin_bit_cast(float, __builtin_amdgcn_readlane(x, 32));
  float d = __builtin_bit_cast(float, __builtin_amdgcn_readlane(x, 48));
  return (a + b) + (c + d);
}
DI u32x4 cmul_pk(f4 re, f4 im, f4 tx, f4 ty) {
  f4 a = re * tx - im * ty, b = re * ty + im * tx;
  return u32x4{pk_h(a[0], b[0]), pk_h(a[1], b[1]), pk_h(a[2], b[2]), pk_h(a[3], b[3])};
}
DI float opq(float v) { asm("" : "+v"(v)); return v; }
DI float h_lo(u32 v) { return (float)__builtin_bit_cast(h2, v)[0]; }
DI float h_hi(u32 v) { return (float)__builtin_bit_cast(h2, v)[1]; }
template <int SG> DI void radix8_half(int e, f4 ar, f4 ai, f4 br, f4 bi, f4& xr, f4& xi) {
  f4 pr, pi;
  if (e == 0) {
    pr = ar + br; pi = ai + bi;
  } else {
    const float r2 = 0.70710678118654752f;
    const f4 cx = {1.f, r2, 0.f, -r2}, cy = {0.f, SG * r2, SG * 1.f, SG * r2};
    f4 dr = ar - br, di = ai - bi;
    pr = dr * cx - di * cy; pi = dr * cy + di * cx;
  }
  f2 sr = pr.xy + pr.zw, si = pi.xy + pi.zw;
  f2 dr = pr.xy - pr.zw, di = pi.xy - pi.zw;
  xr[0] = opq(sr.x + sr.y); xi[0] = opq(si.x + si.y);
  xr[2] = opq(sr.x - sr.y); xi[2] = opq(si.x - si.y);
  if (SG < 0) {
    xr[1] = opq(dr.x + di.y); xi[1] = opq(di.x - dr.y);
    xr[3] = opq(dr.x - di.y); xi[3] = opq(di.x + dr.y);
  } else {
    xr[1] = opq(dr.x - di.y); xi[1] = opq(di.x + dr.y);
    xr[3] = opq(dr.x + di.y); xi[3] = opq(di.x - dr.y);
  }
}
DI f4 mfma_h(u32x4 a, u32x4 b, f4 c) {
  return __builtin_amdgcn_mfma_f32_16x16x32_f16(__builtin_bit_cast(h8, a), __builtin_bit_cast(h8, b), c, 0, 0, 0);
}
DI f4 mfma_b(u32x4 a, u32x4 b, f4 c) {
  return __builtin_amdgcn_mfma_f32_16x16x32_bf16(__builtin_bit_cast(b8, a), __builtin_bit_cast(b8, b), c, 0, 0, 0);
}
DI u32x2 tr_read(const char* p) {
  return __builtin_bit_cast(u32x2, __builtin_amdgcn_ds_read_tr16_b64_v4i16((LDS_AS s4*)p));
}
DI void glds16(const void* g, char* lds_wave_base) {
  __builtin_amdgcn_global_load_lds((const GLB_AS u32*)g, (LDS_AS u32*)lds_wave_base, 16, 0, 0);
}
template <int N> DI void wait_vm() { asm volatile("s_waitcnt vmcnt(%0)" ::"n"(N) : "memory"); }
DI void wait_lgkm0() { asm volatile("s_waitcnt lgkmcnt(0)" ::: "memory"); }
DI void raw_barrier() { __builtin_amdgcn_s_barrier(); }
DI f4 sel4(bool c, f4 a, f4 b) { return f4{c ? a[0] : b[0], c ? a[1] : b[1], c ? a[2] : b[2], c ? a[3] : b[3]}; }
DI int uni(int v) { return __builtin_amdgcn_readfirstlane(v); }

DI f2 ln_scalars(float mean, float var) {
  float m = rbf(mean);
  float v = rbf(var);
  float den = rbf(__builtin_sqrtf(rbf(v + 1e-5f)));
  return f2{m, 1.0f / den};
}
DI float gelu_f(float u) {
  const float c1 = -2.0f * 0.7978845608028654f * 1.4426950408889634f;
  const float c2 = c1 * 0.044715f;
  float e = __builtin_amdgcn_exp2f(u * (c1 + c2 * u * u));
  return u * __builtin_amdgcn_rcpf(1.0f + e);
}
DI f2 w_scales(const u32* wmax, int lane) {
  u32 m = wmax[lane];
  for (int o = 32; o > 0; o >>= 1) { u32 t = __shfl_xor(m, o); m = m > t ? m : t; }
  int e = (int)(m >> 7);
  e = e < 4 ? 4 : (e > 254 ? 254 : e);
  return f2{__builtin_bit_cast(float, (u32)(256 - e) << 23), __builtin_bit_cast(float, (u32)(e - 2) << 23)};
}

constexpr int K1_TAB = 0, K1_XN = 12288, K1_STG = 24576, K1_LDS = 41216;
__global__ __launch_bounds__(512) void k1_fwd(const u32* __restrict__ x, const u32x4* __restrict__ wr,
                                              const u32x4* __restrict__ wi, char* __restrict__ ws) {
  extern __shared__ __attribute__((aligned(16))) char sm[];
  const int tid = threadIdx.x, lane = tid & 63, w = uni(tid >> 6);
  const int n = lane & 15, g = lane >> 4;
  if (blockIdx.x >= 256) {
    const int q = blockIdx.x - 256;
    const int jbase = 8 * (q & 15) + 512 * (q >> 4);
    u32 mx = 0;
    for (int c = tid; c < 8 * 321; c += 512) {
      int part = c / 321, off = c - part * 321;
      const u32x4* src = (part >> 2) ? wi : wr;
      u32x4 v = src[(size_t)((jbase + 128 * (part & 3)) >> 3) * 321 + off];
      *(u32x4*)(sm + c * 16) = v;
#pragma unroll
      for (int i = 0; i < 4; ++i) {
        u32 a = v[i] & 0x7fffu, b = (v[i] >> 16) & 0x7fffu;
        mx = mx > a ? mx : a;
        mx = mx > b ? mx : b;
      }
    }
    for (int o = 32; o > 0; o >>= 1) { u32 t = __shfl_xor(mx, o); mx = mx > t ? mx : t; }
    u32* red = (u32*)(sm + 41088);
    if (lane == 0) red[w] = mx;
    __syncthreads();
    if (tid == 0) {
      u32 m = 0;
      for (int i = 0; i < 8; ++i) m = m > red[i] ? m : red[i];
      ((u32*)(ws + WS_WMAX))[q] = m;
    }
    const u16* s16 = (const u16*)sm;
    u32x4* dst = (u32x4*)(ws + WS_WT);
    for (int c = tid; c < 8 * 321; c += 512) {
      int k = c >> 3, ww = c & 7;
      u32x4 o;
#pragma unroll
      for (int r = 0; r < 4; ++r)
        o[r] = (u32)s16[r * 2568 + ww * 321 + k] | ((u32)s16[(4 + r) * 2568 + ww * 321 + k] << 16);
      dst[(size_t)k * 512 + q * 8 + ww] = o;
    }
    return;
  }
  const int b = blockIdx.x;
  const int rg = (b & 7) * 32 + (b >> 3);
  const int s = rg * 8 + w;
  u32 xv[5];
#pragma unroll
  for (int i = 0; i < 5; ++i) xv[i] = x[(size_t)s * 320 + i * 64 + lane];
  glds16(&g_tab_fwd.v[w][lane][0], sm + K1_TAB + w * 1024);
  if (w < 4) glds16(&g_tab_fwd.v[8 + w][lane][0], sm + K1_TAB + (8 + w) * 1024);
  f2 v[5];
#pragma unroll
  for (int i = 0; i < 5; ++i) v[i] = f2{bf_lo(xv[i]), bf_hi(xv[i])};
  const f2 s2 = ((v[0] + v[1]) + (v[2] + v[3])) + v[4];
  const float mean = wave_sum(opq(s2.x) + opq(s2.y)) * (1.0f / 640.0f);
  f2 q2 = {0.f, 0.f};
#pragma unroll
  for (int i = 0; i < 5; ++i) { f2 d = v[i] - mean; q2 += d * d; }
  const float var = wave_sum(opq(q2.x) + opq(q2.y)) * (1.0f / 640.0f);
  const f2 ln = ln_scalars(mean, var);
  char* xn = sm + K1_XN + w * 1536;
#pragma unroll
  for (int i = 0; i < 5; ++i) {
    u32 d = pk_bf(v[i].x - ln.x, v[i].y - ln.x);
    u32 o = pk_bf(bf_lo(d) * ln.y, bf_hi(d) * ln.y);
    *(u32*)(xn + (i * 64 + lane) * 4) = pk_h(bf_lo(o), bf_hi(o));
  }
  *(u32*)(xn + (320 + lane) * 4) = 0u;
  wait_vm<0>();
  __syncthreads();
  const u32x4* tab = (const u32x4*)(sm + K1_TAB) + lane;
  u32x2 a0 = tr_read(xn + lane * 8), a1 = tr_read(xn + 512 + lane * 8), a2 = tr_read(xn + 1024 + lane * 8);
  u32x4 af = {a0[0], a0[1], a1[0], a1[1]};
  u32x4 tre = tab[4 * 64], tim = tab[5 * 64], fr = tab[10 * 64], fi = tab[11 * 64];
  u32* stg = (u32*)(sm + K1_STG) + w * 328;
#pragma unroll
  for (int nt = 0; nt < 2; ++nt) {
    f4 z = {0.f, 0.f, 0.f, 0.f};
    f4 re = mfma_h(af, tab[(0 + nt) * 64], z);
    f4 im = mfma_h(af, tab[(2 + nt) * 64], z);
    const u32x4 at = nt ? u32x4{0u, 0u, a2[0], a2[1]} : u32x4{a2[0], a2[1], 0u, 0u};
    re = mfma_h(at, tre, re);
    im = mfma_h(at, tim, im);
    u32x4 fb_ = cmul_pk(re, im, __builtin_bit_cast(f4, tab[(6 + 2 * nt) * 64]), __builtin_bit_cast(f4, tab[(7 + 2 * nt) * 64]));
    f4 ore = mfma_h(fr, fb_, z), oim = mfma_h(fi, fb_, z);
    const int k1 = 16 * nt + n;
#pragma unroll
    for (int r = 0; r < 4; ++r) {
      int k2 = 4 * g + r, k = k1 + 40 * k2;
      int kt = k2 < 8 ? k : 640 - k;
      bool ok = k1 <= 20 && !(k1 == 0 && k2 > 8) && !(k1 == 20 && k2 >= 8);
      if (ok) stg[kt] = pk_h(ore[r], oim[r]);
    }
  }
  __syncthreads();
  if (tid < KT) {
    const u32* sp = (const u32*)(sm + K1_STG) + tid;
    u32x4 o0 = {sp[0], sp[328], sp[2 * 328], sp[3 * 328]};
    u32x4 o1 = {sp[4 * 328], sp[5 * 328], sp[6 * 328], sp[7 * 328]};
    u32x4* dst = (u32x4*)(ws + WS_T1) + (size_t)tid * 512 + rg * 2;
    dst[0] = o0;
    dst[1] = o1;
  }
}

constexpr int K2_LDS = 10240 + 16384;
constexpr int K2_NWIN = 68, K2_NWOUT = 34, K2_NSTAT = 8;
DI void transpose16(const u32* __restrict__ src, u32x4* __restrict__ dst, int item, int npair, int nitems) {
  if (item >= nitems) return;
  int kb = item / npair, p = item - kb * npair;
  u32 v[16];
#pragma unroll
  for (int i = 0; i < 16; ++i) v[i] = src[(size_t)(kb * 16 + i) * npair + p];
  u32x4 o[4];
#pragma unroll
  for (int i = 0; i < 8; ++i) {
    o[i >> 2][i & 3] = __builtin_amdgcn_perm(v[2 * i + 1], v[2 * i], 0x05040100u);
    o[2 + (i >> 2)][i & 3] = __builtin_amdgcn_perm(v[2 * i + 1], v[2 * i], 0x07060302u);
  }
  u32x4* d = dst + (size_t)item * 4;
  d[0] = o[0]; d[1] = o[1]; d[2] = o[2]; d[3] = o[3];
}
__global__ __launch_bounds__(512) void k2_col(const u32* __restrict__ w_in, const u32* __restrict__ w_out,
                                              char* __restrict__ ws) {
  extern __shared__ __attribute__((aligned(16))) char sm[];
  const int tid = threadIdx.x, lane = tid & 63, w = uni(tid >> 6);
  const int n = lane & 15, g = lane >> 4;
  if (blockIdx.x >= KT) {
    int q = blockIdx.x - KT;
    if (q < K2_NWIN) {
      transpose16(w_in, (u32x4*)(ws + WS_WIN), q * 512 + tid, F / 2, 40 * (F / 2));
    } else if (q < K2_NWIN + K2_NWOUT) {
      transpose16(w_out, (u32x4*)(ws + WS_WOUT), (q - K2_NWIN) * 512 + tid, D / 2, 54 * (D / 2));
    } else {
      u32x4* st = (u32x4*)(ws + WS_STAT) + (q - K2_NWIN - K2_NWOUT) * 2048 + tid;
      u32x4 f = {~0u, ~0u, ~0u, ~0u};
      st[0] = f; st[512] = f; st[1024] = f; st[1536] = f;
    }
    return;
  }
  const int kt = blockIdx.x;
  const int bp = tid & 255, e = w >> 2;
  const u32* t1 = (const u32*)(ws + WS_T1) + (size_t)kt * 2048 + bp;
  u32 uv[8];
#pragma unroll
  for (int a = 0; a < 8; ++a) uv[a] = t1[a * 256];
  const u32x4 wv = *((const u32x4*)(ws + WS_WT) + (size_t)kt * 512 + lane * 8 + w);
  const f2 wsc = w_scales((const u32*)(ws + WS_WMAX), lane);
  const float rs8 = 0.35355339059327376f;
  f4 tax, tay, t1x, t1y, t3x, t3y;
  u32x4 Fr, Fi, Gr, Gi;
#pragma unroll
  for (int i = 0; i < 4; ++i) {
    float a0 = (float)(bp * (2 * i + e)) * (1.0f / 2048.0f);
    float a1 = (float)((4 * g + i) * n) * (1.0f / 256.0f);
    float a3 = (float)((16 * (4 * g + i) + n) * w) * (1.0f / 2048.0f);
    float a4 = (float)(((4 * g + i) * n) & 15) * (1.0f / 16.0f);
    tax[i] = __builtin_amdgcn_cosf(a0); tay[i] = __builtin_amdgcn_sinf(a0);
    t1x[i] = __builtin_amdgcn_cosf(a1); t1y[i] = __builtin_amdgcn_sinf(a1);
    t3x[i] = __builtin_amdgcn_cosf(a3); t3y[i] = __builtin_amdgcn_sinf(a3);
    float c = __builtin_amdgcn_cosf(a4), sn_ = __builtin_amdgcn_sinf(a4);
    Fr[i] = pk_h(c, sn_); Fi[i] = pk_h(-sn_, c);
    Gr[i] = pk_h(c, -sn_); Gi[i] = pk_h(sn_, c);
  }
  tax *= rs8; tay *= -rs8; t1x *= 0.25f; t1y *= 0.25f; t3x *= 0.25f * rs8; t3y *= 0.25f * rs8;
  {
    f4 ar, ai, br, bi, xr, xi;
#pragma unroll
    for (int a = 0; a < 4; ++a) { ar[a] = h_lo(uv[a]); ai[a] = h_hi(uv[a]); br[a] = h_lo(uv[a + 4]); bi[a] = h_hi(uv[a + 4]); }
    radix8_half<-1>(e, ar, ai, br, bi, xr, xi);
    u32x4 o = cmul_pk(xr, xi, tax, tay);
    u32* ex = (u32*)sm + (bp & 15) * 20 + (bp >> 4) + e * 320;
#pragma unroll
    for (int m = 0; m < 4; ++m) ex[m * 640] = o[m];
  }
  __syncthreads();
  const f4 z = {0.f, 0.f, 0.f, 0.f};
  u32x4 fa = *(const u32x4*)(sm + (w * 320 + n * 20 + 4 * g) * 4);
  f4 re = mfma_h(fa, Fr, z), im = mfma_h(fa, Fi, z);
  fa = cmul_pk(re, im, t1x, -t1y);
  re = mfma_h(Fr, fa, z); im = mfma_h(Fi, fa, z);
  {
    const float wk = 0.25f * wsc.x;
    f4 wx, wy;
#pragma unroll
    for (int r = 0; r < 4; ++r) { wx[r] = bf_lo(wv[r]); wy[r] = bf_hi(wv[r]); }
    fa = cmul_pk(re, im, wx * wk, wy * wk);
  }
  re = mfma_h(fa, Gr, z); im = mfma_h(fa, Gi, z);
  fa = cmul_pk(re, im, t1x, t1y);
  re = mfma_h(Gr, fa, z); im = mfma_h(Gi, fa, z);
  {
    float* ex = (float*)(sm + 10240) + w * 256 + 64 * g + n;
    f4 a = re * t3x - im * t3y, b = re * t3y + im * t3x;
#pragma unroll
    for (int r = 0; r < 4; ++r) { ex[16 * r] = a[r]; ex[2048 + 16 * r] = b[r]; }
  }
  __syncthreads();
  {
    const float* ex = (const float*)(sm + 10240) + bp;
    f4 ar, ai, br, bi, xr, xi;
#pragma unroll
    for (int j = 0; j < 4; ++j) {
      ar[j] = ex[j * 256]; br[j] = ex[(j + 4) * 256];
      ai[j] = ex[2048 + j * 256]; bi[j] = ex[2048 + (j + 4) * 256];
    }
    radix8_half<1>(e, ar, ai, br, bi, xr, xi);
    u32* t2p = (u32*)(ws + WS_T2) + (size_t)kt * 2048 + bp + e * 256;
#pragma unroll
    for (int m = 0; m < 4; ++m) t2p[m * 512] = pk_h(xr[m], xi[m]);
  }
}

constexpr int K3_TAB = 0, K3_Z = 12288, K3_ZSTR = 340, K3_LDS = 12288 + 8 * K3_ZSTR * 4;
__global__ __launch_bounds__(512) void k3_inv(const u32x2* __restrict__ x, char* __restrict__ ws) {
  extern __shared__ __attribute__((aligned(16))) char sm[];
  const int tid = threadIdx.x, lane = tid & 63, w = uni(tid >> 6);
  const int n = lane & 15, g = lane >> 4;
  const int b = blockIdx.x;
  const int rg = (b & 7) * 32 + (b >> 3);
  const int s = rg * 8 + w;
  u32x4 z0, z1;
  if (tid < 336) {
    int k1 = tid >> 4, k2 = tid & 15, k = k1 + 40 * k2;
    int kt = k2 < 8 ? k : 640 - k;
    const u32x4* src = (const u32x4*)(ws + WS_T2) + (size_t)kt * 512 + rg * 2;
    z0 = src[0]; z1 = src[1];
  }
  u32x2 xv[3];
#pragma unroll
  for (int nt = 0; nt < 3; ++nt) {
    int a = 16 * nt + n;
    a = a < 40 ? a : 39;
    xv[nt] = x[(size_t)s * 160 + a * 4 + g];
  }
  const f2 wsc = w_scales((const u32*)(ws + WS_WMAX), lane);
  glds16(&g_tab_inv.v[w][lane][0], sm + K3_TAB + w * 1024);
  if (w < 4) glds16(&g_tab_inv.v[8 + w][lane][0], sm + K3_TAB + (8 + w) * 1024);
  if (tid < 336) {
    u32* zp = (u32*)(sm + K3_Z) + tid;
#pragma unroll
    for (int i = 0; i < 4; ++i) { zp[i * K3_ZSTR] = z0[i]; zp[(4 + i) * K3_ZSTR] = z1[i]; }
  }
  wait_vm<0>();
  __syncthreads();
  const u32x4* tab = (const u32x4*)(sm + K3_TAB) + lane;
  const f4 z = {0.f, 0.f, 0.f, 0.f};
  u32x4 gr = tab[0], gi = tab[64];
  u32x4 fv[2];
#pragma unroll
  for (int mt = 0; mt < 2; ++mt) {
    int k1 = 16 * mt + n;
    k1 = k1 <= 20 ? k1 : 20;
    u32x4 fa = *(const u32x4*)(sm + K3_Z + (w * K3_ZSTR + k1 * 16 + 4 * g) * 4);
    f4 re = mfma_h(fa, gr, z), im = mfma_h(fa, gi, z);
    fv[mt] = cmul_pk(re, im, __builtin_bit_cast(f4, tab[(2 + 2 * mt) * 64]), __builtin_bit_cast(f4, tab[(3 + 2 * mt) * 64]));
  }
  float hv[12];
  u32x2 hp[3];
  float sum = 0.f;
  const bool v2 = n < 8;
#pragma unroll
  for (int nt = 0; nt < 3; ++nt) {
    f4 y = mfma_h(fv[0], tab[(6 + 2 * nt) * 64], z);
    y = mfma_h(fv[1], tab[(7 + 2 * nt) * 64], y);
    u32 y0 = pk_bf(y[0] * wsc.y, y[1] * wsc.y), y1 = pk_bf(y[2] * wsc.y, y[3] * wsc.y);
    hp[nt][0] = pk_bf(bf_lo(xv[nt][0]) + bf_lo(y0), bf_hi(xv[nt][0]) + bf_hi(y0));
    hp[nt][1] = pk_bf(bf_lo(xv[nt][1]) + bf_lo(y1), bf_hi(xv[nt][1]) + bf_hi(y1));
    hv[4 * nt + 0] = bf_lo(hp[nt][0]); hv[4 * nt + 1] = bf_hi(hp[nt][0]);
    hv[4 * nt + 2] = bf_lo(hp[nt][1]); hv[4 * nt + 3] = bf_hi(hp[nt][1]);
    float t = (hv[4 * nt] + hv[4 * nt + 1]) + (hv[4 * nt + 2] + hv[4 * nt + 3]);
    sum += (nt < 2 || v2) ? t : 0.f;
  }
  const float mean = wave_sum(sum) * (1.0f / 640.0f);
  float sq = 0.f;
#pragma unroll
  for (int nt = 0; nt < 3; ++nt) {
    float t = 0.f;
#pragma unroll
    for (int r = 0; r < 4; ++r) { float d = hv[4 * nt + r] - mean; t += d * d; }
    sq += (nt < 2 || v2) ? t : 0.f;
  }
  const float var = wave_sum(sq) * (1.0f / 640.0f);
  const f2 ln = ln_scalars(mean, var);
  u32x2* hb = (u32x2*)(ws + WS_H);
  u32x2* hn = (u32x2*)(ws + WS_HN);
#pragma unroll
  for (int nt = 0; nt < 3; ++nt) {
    if (nt < 2 || v2) {
      u32 d0 = pk_bf(hv[4 * nt] - ln.x, hv[4 * nt + 1] - ln.x), d1 = pk_bf(hv[4 * nt + 2] - ln.x, hv[4 * nt + 3] - ln.x);
      u32x2 o = {pk_bf(bf_lo(d0) * ln.y, bf_hi(d0) * ln.y), pk_bf(bf_lo(d1) * ln.y, bf_hi(d1) * ln.y)};
      size_t idx = ((size_t)(16 * nt + n) * 2048 + s) * 4 + g;
      hb[idx] = hp[nt];
      hn[idx] = o;
    }
  }
}

DI void tile_map(int& rt, int& cg) {
  int b = blockIdx.x, x = b & 7, i = b >> 3;
  rt = (x >> 1) * 8 + (i >> 2);
  cg = (x & 1) * 4 + (i & 3);
}
DI int cg_start(int cg) { return cg * 7 - (cg >> 2); }
DI int cg_count(int cg) { return (cg & 3) == 3 ? 6 : 7; }

template <int I, int N, class Fn> DI void static_for(Fn&& f) {
  if constexpr (I < N) { f(std::integral_constant<int, I>{}); static_for<I + 1, N>(f); }
}

constexpr int G1_NS = 8, G1_STG = 14336, G1_RED = G1_NS * G1_STG, G1_LDS = G1_RED + 32768;
__global__ __launch_bounds__(512, 2) void k4_gemm1(char* __restrict__ ws) {
  extern __shared__ __attribute__((aligned(16))) char sm[];
  const int tid = threadIdx.x, lane = tid & 63, w = uni(tid >> 6);
  const int n = lane & 15, g = lane >> 4;
  int rt, cg;
  tile_map(rt, cg);
  const int nt_all = cg_count(cg), t0 = cg_start(cg);
  const int kg = w >> 2, mh = (w >> 1) & 1, np = (w ^ (w >> 2)) & 1;
  const int nfirst = np ? nt_all - 3 : 0, ncnt = np ? 3 : nt_all - 3;
  const int rows = nt_all * 16, per = rows >> 1;
  const char* bsrc[2];
#pragma unroll
  for (int i = 0; i < 2; ++i) {
    int c = (2 * w + i) * per + (lane < per ? lane : 0);
    int kb = c / (2 * rows), rem = c - kb * 2 * rows;
    bsrc[i] = ws + WS_WIN + ((size_t)kb * F + t0 * 16) * 32 + rem * 16;
  }
  auto issue = [&](int st, int slot) {
    size_t off = st < 10 ? (size_t)st * (4 * F * 32) + FH * 32 : (size_t)(st - 10) * (4 * F * 32);
    if (lane < per) {
      glds16(bsrc[0] + off, sm + slot * G1_STG + (2 * w) * per * 16);
      glds16(bsrc[1] + off, sm + slot * G1_STG + (2 * w + 1) * per * 16);
    }
  };
  u32x4 af[10][2];
  {
    const char* ap = ws + WS_HN + ((size_t)(2 * kg + (g >> 1)) * 2048 + rt * 64 + mh * 32 + n) * 32 + (g & 1) * 16;
#pragma unroll
    for (int st = 0; st < 10; ++st)
#pragma unroll
      for (int m = 0; m < 2; ++m) af[st][m] = *(const u32x4*)(ap + (size_t)st * (4 * 2048 * 32) + m * 512);
  }
#pragma unroll
  for (int st = 0; st < G1_NS - 1; ++st) issue(st, st);
  const int boff = (2 * kg + (g >> 1)) * rows * 32 + (nfirst * 16 + n) * 32 + (g & 1) * 16;
  f4 acc[4][2];
  u32 z2p[4][2];
  const int srow = rt * 64 + (2 * mh + kg) * 16;
  auto reduce = [&]() {
    f4* red = (f4*)(sm + G1_RED);
#pragma unroll
    for (int j = 0; j < 4; ++j) red[(w * 4 + j) * 64 + lane] = sel4(kg, acc[j][0], acc[j][1]);
    wait_lgkm0(); raw_barrier();
    const int pw = (w ^ 4) ^ 1;
#pragma unroll
    for (int j = 0; j < 4; ++j) acc[j][0] = sel4(kg, acc[j][1], acc[j][0]) + red[(pw * 4 + j) * 64 + lane];
  };
  static_for<0, 20>([&](auto ST) {
    constexpr int st = ST.value;
    constexpr int left = (19 - st) < (G1_NS - 2) ? (19 - st) : (G1_NS - 2);
    if constexpr (st == 0 || st == 10) {
#pragma unroll
      for (int j = 0; j < 4; ++j) { acc[j][0] = f4{0.f, 0.f, 0.f, 0.f}; acc[j][1] = f4{0.f, 0.f, 0.f, 0.f}; }
    }
    wait_vm<2 * left>(); wait_lgkm0(); raw_barrier();
    if constexpr (st + G1_NS - 1 < 20) issue(st + G1_NS - 1, (st + G1_NS - 1) % G1_NS);
    const char* bp = sm + (st % G1_NS) * G1_STG + boff;
#pragma unroll
    for (int j = 0; j < 4; ++j) {
      if (j < ncnt) {
        u32x4 bf = *(const u32x4*)(bp + j * 512);
        if constexpr (st < 10) {
          acc[j][0] = mfma_b(af[st][0], bf, acc[j][0]);
          acc[j][1] = mfma_b(af[st][1], bf, acc[j][1]);
        } else {
          acc[j][0] = mfma_b(bf, af[st - 10][0], acc[j][0]);
          acc[j][1] = mfma_b(bf, af[st - 10][1], acc[j][1]);
        }
      }
    }
    if constexpr (st == 9) {
      reduce();
      float zv[4][4], sum[4] = {0.f, 0.f, 0.f, 0.f};
#pragma unroll
      for (int j = 0; j < 4; ++j) {
        u32 u0 = pk_bf(acc[j][0][0], acc[j][0][1]), u1 = pk_bf(acc[j][0][2], acc[j][0][3]);
        z2p[j][0] = pk_bf(gelu_f(bf_lo(u0)), gelu_f(bf_hi(u0)));
        z2p[j][1] = pk_bf(gelu_f(bf_lo(u1)), gelu_f(bf_hi(u1)));
        zv[j][0] = bf_lo(z2p[j][0]); zv[j][1] = bf_hi(z2p[j][0]);
        zv[j][2] = bf_lo(z2p[j][1]); zv[j][3] = bf_hi(z2p[j][1]);
#pragma unroll
        for (int r = 0; r < 4; ++r) sum[r] += (j < ncnt) ? zv[j][r] : 0.f;
      }
      const float inv = 1.0f / (float)(ncnt * 16);
      float mean[4], m2[4];
#pragma unroll
      for (int r = 0; r < 4; ++r) {
        mean[r] = row16_sum(sum[r]) * inv;
        float q = 0.f;
#pragma unroll
        for (int j = 0; j < 4; ++j) { float d = zv[j][r] - mean[r]; q += (j < ncnt) ? d * d : 0.f; }
        m2[r] = row16_sum(q);
      }
      float mm = n == 0 ? mean[0] : n == 1 ? mean[1] : n == 2 ? mean[2] : mean[3];
      float qq = n == 0 ? m2[0] : n == 1 ? m2[1] : n == 2 ? m2[2] : m2[3];
      if (n < 4) {
        u64 gr = (u64)__builtin_bit_cast(u32, mm) | ((u64)(__builtin_bit_cast(u32, qq) & 0x7fffffffu) << 32);
        __hip_atomic_store((GLB_AS u64*)(ws + WS_STAT) + (size_t)(srow + 4 * g + n) * 16 + cg * 2 + np, gr,
                           __ATOMIC_RELAXED, __HIP_MEMORY_SCOPE_AGENT);
      }
    }
  });
  wait_lgkm0(); raw_barrier();
  reduce();
  {
    u32x2* z1 = (u32x2*)(ws + WS_Z1);
#pragma unroll
    for (int j = 0; j < 4; ++j) {
      if (j < ncnt) {
        u32 u0 = pk_bf(acc[j][0][0], acc[j][0][1]), u1 = pk_bf(acc[j][0][2], acc[j][0][3]);
        u32x2 o = {pk_bf(gelu_f(bf_lo(u0)), gelu_f(bf_hi(u0))), pk_bf(gelu_f(bf_lo(u1)), gelu_f(bf_hi(u1)))};
        z1[((size_t)(t0 + nfirst + j) * 2048 + srow + n) * 4 + g] = o;
      }
    }
  }
  {
    const GLB_AS u64* sp = (const GLB_AS u64*)(ws + WS_STAT) + (size_t)(srow + 4 * g) * 16 + n;
    u64 gr[4];
    for (int spin = 0; spin < (1 << 22); ++spin) {
      bool ok = true;
#pragma unroll
      for (int r = 0; r < 4; ++r) {
        gr[r] = __hip_atomic_load(sp + r * 16, __ATOMIC_RELAXED, __HIP_MEMORY_SCOPE_AGENT);
        ok = ok && !(gr[r] >> 63);
      }
      if (__all(ok)) break;
      __builtin_amdgcn_s_sleep(1);
    }
    const float cnt = (n & 1) ? 48.f : (((n >> 1) & 3) == 3 ? 48.f : 64.f);
    f2 ln[4];
#pragma unroll
    for (int r = 0; r < 4; ++r) {
      float mp = __builtin_bit_cast(float, (u32)gr[r]), qp = __builtin_bit_cast(float, (u32)(gr[r] >> 32));
      float mean = row16_sum(mp * cnt) * (1.0f / 864.0f);
      float d = mp - mean;
      float var = row16_sum(qp + cnt * d * d) * (1.0f / 864.0f);
      ln[r] = ln_scalars(mean, var);
    }
    u32x2* z2 = (u32x2*)(ws + WS_Z2);
#pragma unroll
    for (int j = 0; j < 4; ++j) {
      if (j < ncnt) {
        u32 d0 = pk_bf(bf_lo(z2p[j][0]) - ln[0].x, bf_hi(z2p[j][0]) - ln[1].x);
        u32 d1 = pk_bf(bf_lo(z2p[j][1]) - ln[2].x, bf_hi(z2p[j][1]) - ln[3].x);
        u32x2 o = {pk_bf(bf_lo(d0) * ln[0].y, bf_hi(d0) * ln[1].y), pk_bf(bf_lo(d1) * ln[2].y, bf_hi(d1) * ln[3].y)};
        z2[((size_t)(srow >> 4) * FH + (t0 + nfirst + j) * 16 + n) * 4 + g] = o;
      }
    }
  }
}

constexpr int G2_NS = 7, G2_A = 14336, G2_STG = 14336 + 8192, G2_LDS = G2_NS * G2_STG;
__global__ __launch_bounds__(512, 2) void k5_gemm2(const char* __restrict__ wsp, const u16* __restrict__ bsp,
                                                   char* __restrict__ ws) {
  extern __shared__ __attribute__((aligned(16))) char sm[];
  const int tid = threadIdx.x, lane = tid & 63, w = uni(tid >> 6);
  const int n = lane & 15, g = lane >> 4;
  int rt, cg;
  tile_map(rt, cg);
  const int nt_all = cg_count(cg), t0 = cg_start(cg);
  const int kg = w >> 2, mh = (w >> 1) & 1, np = (w ^ (w >> 2)) & 1;
  const int nfirst = np ? nt_all - 3 : 0, ncnt = np ? 3 : nt_all - 3;
  const int rows = nt_all * 16, per = rows >> 1;
  const char* asrc[2];
#pragma unroll
  for (int i = 0; i < 2; ++i) {
    int c = (2 * w + i) * per + (lane < per ? lane : 0);
    int kb = c / (2 * rows), rem = c - kb * 2 * rows;
    asrc[i] = ws + WS_Z2 + ((size_t)kb * FH + t0 * 16) * 32 + rem * 16;
  }
  const int brow = w * 8 + (lane >> 3);
  const char* bsrc = wsp + (size_t)(rt * 64 + brow) * 4096 + (((lane & 7) ^ ((brow >> 1) & 7)) << 4);
  const int trow = rt * 64 + (2 * mh + kg) * 16 + n;
  u32x2 z1v[4];
#pragma unroll
  for (int j = 0; j < 4; ++j) {
    int jj = j < ncnt ? j : 0;
    z1v[j] = ((const u32x2*)(ws + WS_Z1))[((size_t)(t0 + nfirst + jj) * 2048 + trow) * 4 + g];
  }
  const float bias = bf_lo(bsp[trow]);
  auto issue = [&](int st, int slot) {
    char* base = sm + slot * G2_STG;
    glds16(bsrc + st * 128, base + G2_A + w * 1024);
    if (lane < per) {
      glds16(asrc[0] + (size_t)st * (4 * FH * 32), base + (2 * w) * per * 16);
      glds16(asrc[1] + (size_t)st * (4 * FH * 32), base + (2 * w + 1) * per * 16);
    }
  };
#pragma unroll
  for (int st = 0; st < G2_NS - 1; ++st) issue(st, st);
  const int aoff = (2 * kg + (g >> 1)) * rows * 32 + (nfirst * 16 + n) * 32 + (g & 1) * 16;
  int boff[2];
#pragma unroll
  for (int m = 0; m < 2; ++m) {
    int r = mh * 32 + m * 16 + n;
    boff[m] = G2_A + r * 128 + (((4 * kg + g) ^ ((r >> 1) & 7)) << 4);
  }
  f4 acc[4][2];
#pragma unroll
  for (int j = 0; j < 4; ++j) { acc[j][0] = f4{0.f, 0.f, 0.f, 0.f}; acc[j][1] = f4{0.f, 0.f, 0.f, 0.f}; }
  auto comp = [&](int slot) {
    const char* base = sm + slot * G2_STG;
    u32x4 b0 = *(const u32x4*)(base + boff[0]), b1 = *(const u32x4*)(base + boff[1]);
#pragma unroll
    for (int j = 0; j < 4; ++j) {
      if (j < ncnt) {
        u32x4 a = *(const u32x4*)(base + aoff + j * 512);
        acc[j][0] = mfma_b(a, b0, acc[j][0]);
        acc[j][1] = mfma_b(a, b1, acc[j][1]);
      }
    }
  };
  int cur = 0, nxt = G2_NS - 1;
  for (int st = 0; st < 32 - (G2_NS - 1); ++st) {
    wait_vm<3 * (G2_NS - 2)>(); wait_lgkm0(); raw_barrier();
    issue(st + G2_NS - 1, nxt);
    comp(cur);
    cur = cur + 1 == G2_NS ? 0 : cur + 1;
    nxt = nxt + 1 == G2_NS ? 0 : nxt + 1;
  }
  static_for<0, G2_NS - 1>([&](auto I) {
    wait_vm<3 * (G2_NS - 2 - I.value)>(); wait_lgkm0(); raw_barrier();
    comp(cur);
    cur = cur + 1 == G2_NS ? 0 : cur + 1;
  });
  wait_lgkm0(); raw_barrier();
  f4* red = (f4*)sm;
#pragma unroll
  for (int j = 0; j < 4; ++j) red[(w * 4 + j) * 64 + lane] = sel4(kg, acc[j][0], acc[j][1]);
  wait_lgkm0(); raw_barrier();
  const int pw = (w ^ 4) ^ 1;
  u32x2* P = (u32x2*)(ws + WS_P);
#pragma unroll
  for (int j = 0; j < 4; ++j) {
    if (j < ncnt) {
      f4 v = sel4(kg, acc[j][1], acc[j][0]) + red[(pw * 4 + j) * 64 + lane];
      u32 g0 = pk_bf(v[0], v[1]), g1 = pk_bf(v[2], v[3]);
      g0 = pk_bf(bf_lo(g0) + bias, bf_hi(g0) + bias);
      g1 = pk_bf(bf_lo(g1) + bias, bf_hi(g1) + bias);
      u32x2 o = {pk_bf(bf_lo(g0) * bf_lo(z1v[j][0]), bf_hi(g0) * bf_hi(z1v[j][0])),
                 pk_bf(bf_lo(g1) * bf_lo(z1v[j][1]), bf_hi(g1) * bf_hi(z1v[j][1]))};
      P[((size_t)(t0 + nfirst + j) * 2048 + trow) * 4 + g] = o;
    }
  }
}

constexpr int G3_NS = 8, G3_A = 10240, G3_STG = 10240 + 8192, G3_LDS = G3_NS * G3_STG;
__global__ __launch_bounds__(512, 2) void k6_gemm3(char* __restrict__ ws, u32x2* __restrict__ out) {
  extern __shared__ __attribute__((aligned(16))) char sm[];
  const int tid = threadIdx.x, lane = tid & 63, w = uni(tid >> 6);
  const int n = lane & 15, g = lane >> 4;
  int rt, cg;
  tile_map(rt, cg);
  const int kg = w >> 2, mh = (w >> 1) & 1, np = (w ^ (w >> 2)) & 1;
  const int nfirst = np ? 3 : 0, ncnt = np ? 2 : 3;
  const int akb = w >> 1;
  const char* asrc = ws + WS_WOUT + ((size_t)cg * 80) * 32 + ((w & 1) * 80 + (lane < 40 ? lane : 0)) * 16;
  const char* bsrc = ws + WS_P + ((size_t)rt * 64) * 32 + ((w & 1) * 64 + lane) * 16;
  const int trow = rt * 64 + (2 * mh + kg) * 16 + n;
  u32x2 hv[3];
#pragma unroll
  for (int j = 0; j < 3; ++j) {
    int jj = j < ncnt ? j : 0;
    hv[j] = ((const u32x2*)(ws + WS_H))[((size_t)(cg * 5 + nfirst + jj) * 2048 + trow) * 4 + g];
  }
  auto issue = [&](int st, int slot) {
    char* base = sm + slot * G3_STG;
    int kb = st * 4 + akb;
    kb = kb < 54 ? kb : 53;
    glds16(bsrc + (size_t)kb * (2048 * 32), base + G3_A + w * 1024);
    if (lane < 40) {
      glds16(asrc + (size_t)kb * (D * 32), base + w * 1280);
      glds16(asrc + (size_t)kb * (D * 32) + 640, base + w * 1280 + 640);
    }
  };
#pragma unroll
  for (int st = 0; st < G3_NS - 1; ++st) issue(st, st);
  const int aoff = (2 * kg + (g >> 1)) * 2560 + (nfirst * 16 + n) * 32 + (g & 1) * 16;
  const int boff = G3_A + (2 * kg + (g >> 1)) * 2048 + (mh * 32 + n) * 32 + (g & 1) * 16;
  f4 acc[3][2];
#pragma unroll
  for (int j = 0; j < 3; ++j) { acc[j][0] = f4{0.f, 0.f, 0.f, 0.f}; acc[j][1] = f4{0.f, 0.f, 0.f, 0.f}; }
  static_for<0, 14>([&](auto ST) {
    constexpr int st = ST.value;
    constexpr int left = (13 - st) < (G3_NS - 2) ? (13 - st) : (G3_NS - 2);
    wait_vm<3 * left>(); wait_lgkm0(); raw_barrier();
    if constexpr (st + G3_NS - 1 < 14) issue(st + G3_NS - 1, (st + G3_NS - 1) % G3_NS);
    if (st < 13 || kg == 0) {
      const char* base = sm + (st % G3_NS) * G3_STG;
      u32x4 b0 = *(const u32x4*)(base + boff), b1 = *(const u32x4*)(base + boff + 512);
#pragma unroll
      for (int j = 0; j < 3; ++j) {
        if (j < ncnt) {
          u32x4 a = *(const u32x4*)(base + aoff + j * 512);
          acc[j][0] = mfma_b(a, b0, acc[j][0]);
          acc[j][1] = mfma_b(a, b1, acc[j][1]);
        }
      }
    }
  });
  wait_lgkm0(); raw_barrier();
  f4* red = (f4*)sm;
#pragma unroll
  for (int j = 0; j < 3; ++j) red[(w * 4 + j) * 64 + lane] = sel4(kg, acc[j][0], acc[j][1]);
  wait_lgkm0(); raw_barrier();
  const int pw = (w ^ 4) ^ 1;
#pragma unroll
  for (int j = 0; j < 3; ++j) {
    if (j < ncnt) {
      f4 v = sel4(kg, acc[j][1], acc[j][0]) + red[(pw * 4 + j) * 64 + lane];
      u32 o0 = pk_bf(v[0], v[1]), o1 = pk_bf(v[2], v[3]);
      u32x2 o = {pk_bf(bf_lo(hv[j][0]) + bf_lo(o0), bf_hi(hv[j][0]) + bf_hi(o0)),
                 pk_bf(bf_lo(hv[j][1]) + bf_lo(o1), bf_hi(hv[j][1]) + bf_hi(o1))};
      out[(size_t)trow * 160 + (cg * 5 + nfirst + j) * 4 + g] = o;
    }
  }
}

extern "C" void kernel_launch(void* const* d_in, const int* in_sizes, int n_in, void* d_out, int out_size,
                              void* d_ws, size_t ws_size, hipStream_t stream) {
  (void)in_sizes; (void)n_in; (void)out_size; (void)ws_size;
  char* ws = (char*)d_ws;
  (void)hipFuncSetAttribute((const void*)k4_gemm1, hipFuncAttributeMaxDynamicSharedMemorySize, G1_LDS);
  (void)hipFuncSetAttribute((const void*)k5_gemm2, hipFuncAttributeMaxDynamicSharedMemorySize, G2_LDS);
  (void)hipFuncSetAttribute((const void*)k6_gemm3, hipFuncAttributeMaxDynamicSharedMemorySize, G3_LDS);
  k1_fwd<<<256 + 64, 512, K1_LDS, stream>>>((const u32*)d_in[0], (const u32x4*)d_in[1], (const u32x4*)d_in[2], ws);
  k2_col<<<KT + K2_NWIN + K2_NWOUT + K2_NSTAT, 512, K2_LDS, stream>>>((const u32*)d_in[4], (const u32*)d_in[7], ws);
  k3_inv<<<256, 512, K3_LDS, stream>>>((const u32x2*)d_in[0], ws);
  k4_gemm1<<<256, 512, G1_LDS, stream>>>(ws);
  k5_gemm2<<<256, 512, G2_LDS, stream>>>((const char*)d_in[5], (const u16*)d_in[6], ws);
  k6_gemm3<<<256, 512, G3_LDS, stream>>>(ws, (u32x2*)d_out);
}
```

```cpp
#include <hip/hip_runtime.h>
#include <stdint.h>

typedef unsigned int u32;
typedef unsigned short u16;
typedef unsigned long long u64;
typedef _Float16 h2 __attribute__((ext_vector_type(2)));
typedef _Float16 h4 __attribute__((ext_vector_type(4)));
typedef _Float16 h8 __attribute__((ext_vector_type(8)));
typedef short s4 __attribute__((ext_vector_type(4)));
typedef __bf16 b2 __attribute__((ext_vector_type(2)));
typedef __bf16 b8 __attribute__((ext_vector_type(8)));
typedef float f2 __attribute__((ext_vector_type(2)));
typedef float f4 __attribute__((ext_vector_type(4)));
typedef u32 u32x2 __attribute__((ext_vector_type(2)));
typedef u32 u32x4 __attribute__((ext_vector_type(4)));
#define DI __device__ __forceinline__
#define LDS_AS __attribute__((address_space(3)))
#define GLB_AS __attribute__((address_space(1)))

constexpr int S = 2048, D = 640, FH = 864, F = 1728, KT = 321;

constexpr size_t MB = 1u << 20;
constexpr size_t WS_T1 = 0 * MB;
constexpr size_t WS_T2 = 3 * MB;
constexpr size_t WS_WT = 6 * MB;
constexpr size_t WS_H = 9 * MB;
constexpr size_t WS_HN = 12 * MB;
constexpr size_t WS_WIN = 15 * MB;
constexpr size_t WS_WOUT = 18 * MB;
constexpr size_t WS_Z1 = 20 * MB;
constexpr size_t WS_Z2 = 24 * MB;
constexpr size_t WS_P = 28 * MB;
constexpr size_t WS_STAT = 32 * MB;
constexpr size_t WS_WMAX = 33 * MB;

constexpr double kPi = 3.14159265358979323846264338327950288;
constexpr double c_sin_small(double x) {
  double t = x, s = x;
  for (int i = 1; i < 10; ++i) { t *= -x * x / ((2 * i) * (2 * i + 1)); s += t; }
  return s;
}
constexpr double c_cos_small(double x) {
  double t = 1, s = 1;
  for (int i = 1; i < 10; ++i) { t *= -x * x / ((2 * i - 1) * (2 * i)); s += t; }
  return s;
}
struct Cos640 { double c[640]; };
constexpr Cos640 make_cos640() {
  Cos640 t{};
  for (int j = 0; j <= 160; ++j) {
    double v = (j <= 80) ? c_cos_small(2 * kPi * j / 640) : c_sin_small(2 * kPi * (160 - j) / 640);
    if (j == 160) v = 0.0;
    t.c[j] = v;
    t.c[(640 - j) % 640] = v;
    t.c[320 - j] = -v;
    t.c[(320 + j) % 640] = -v;
  }
  return t;
}
constexpr Cos640 kCos = make_cos640();
constexpr double cs(int j) { return kCos.c[((j % 640) + 640) % 640]; }
constexpr double sn(int j) { return kCos.c[(((j - 160) % 640) + 640) % 640]; }
constexpr u32 pkh(double lo, double hi) {
  return (u32)__builtin_bit_cast(u16, (_Float16)(float)lo) | ((u32)__builtin_bit_cast(u16, (_Float16)(float)hi) << 16);
}
constexpr u32 fb(double v) { return __builtin_bit_cast(u32, (float)v); }
constexpr double kRs40 = 0.15811388300841896659994467722163593;

struct Tab { u32 v[12][64][4]; };
constexpr Tab make_tab_fwd() {
  Tab t{};
  for (int l = 0; l < 64; ++l) {
    int n = l & 15, g = l >> 4;
    for (int nt = 0; nt < 2; ++nt) {
      int k1 = 16 * nt + n;
      bool ok = k1 <= 20;
      for (int q = 0; q < 4; ++q) {
        int a0 = (q < 2) ? 4 * g + 2 * q : 16 + 4 * g + 2 * (q - 2), a1 = a0 + 1;
        t.v[0 + nt][l][q] = ok ? pkh(cs(16 * a0 * k1), cs(16 * a1 * k1)) : 0u;
        t.v[2 + nt][l][q] = ok ? pkh(-sn(16 * a0 * k1), -sn(16 * a1 * k1)) : 0u;
      }
      for (int q = 0; q < 2; ++q) {
        int a0 = 32 + 4 * g + 2 * q, a1 = a0 + 1;
        bool v = ok && a0 < 40;
        t.v[4][l][2 * nt + q] = v ? pkh(cs(16 * a0 * k1), cs(16 * a1 * k1)) : 0u;
        t.v[5][l][2 * nt + q] = v ? pkh(-sn(16 * a0 * k1), -sn(16 * a1 * k1)) : 0u;
      }
      for (int r = 0; r < 4; ++r) {
        int b = 4 * g + r;
        t.v[6 + 2 * nt][l][r] = fb(cs(b * k1) * kRs40);
        t.v[7 + 2 * nt][l][r] = fb(-sn(b * k1) * kRs40);
      }
    }
    for (int q = 0; q < 4; ++q) {
      int b = 4 * g + q, k2 = n;
      double c = cs(40 * b * k2) * 0.25, s = sn(40 * b * k2) * 0.25, sg = (k2 >= 8) ? -1.0 : 1.0;
      t.v[10][l][q] = pkh(c, s);
      t.v[11][l][q] = pkh(-s * sg, c * sg);
    }
  }
  return t;
}
constexpr Tab make_tab_inv() {
  Tab t{};
  for (int l = 0; l < 64; ++l) {
    int n = l & 15, g = l >> 4;
    for (int q = 0; q < 4; ++q) {
      int k2 = 4 * g + q, b = n;
      double c = cs(40 * b * k2) * 0.25, s = sn(40 * b * k2) * 0.25, sg = (k2 >= 8) ? -1.0 : 1.0;
      t.v[0][l][q] = pkh(c, -s * sg);
      t.v[1][l][q] = pkh(s, c * sg);
    }
    for (int mt = 0; mt < 2; ++mt) {
      for (int r = 0; r < 4; ++r) {
        int k1 = 16 * mt + 4 * g + r, b = n;
        t.v[2 + 2 * mt][l][r] = fb(cs(b * k1) * kRs40);
        t.v[3 + 2 * mt][l][r] = fb(sn(b * k1) * kRs40);
      }
      for (int nt = 0; nt < 3; ++nt) {
        int a = 16 * nt + n;
        for (int q = 0; q < 4; ++q) {
          int k1 = 16 * mt + 4 * g + q;
          double w = (k1 == 0 || k1 == 20) ? 1.0 : 2.0;
          bool ok = k1 <= 20 && a < 40;
          t.v[6 + 2 * nt + mt][l][q] = ok ? pkh(w * cs(16 * a * k1), -w * sn(16 * a * k1)) : 0u;
        }
      }
    }
  }
  return t;
}
static __device__ constexpr Tab g_tab_fwd = make_tab_fwd();
static __device__ constexpr Tab g_tab_inv = make_tab_inv();

#define STAMP_DECL do {} while (0)
#define STAMP(I) do {} while (0)
#define STAMP_OUT(K) do {} while (0)
DI float bf_lo(u32 v) { return __builtin_bit_cast(float, v << 16); }
DI float bf_hi(u32 v) { return __builtin_bit_cast(float, v & 0xffff0000u); }
DI u32 pk_bf(float a, float b) { f2 x = {a, b}; return __builtin_bit_cast(u32, __builtin_convertvector(x, b2)); }
DI u32 pk_h(float a, float b) { f2 x = {a, b}; return __builtin_bit_cast(u32, __builtin_convertvector(x, h2)); }
DI float rbf(float a) { return bf_lo(pk_bf(a, a)); }
template <int CTRL> DI float dpp(float v) {
  return __builtin_bit_cast(float, __builtin_amdgcn_update_dpp(0, __builtin_bit_cast(int, v), CTRL, 0xf, 0xf, false));
}
DI float row16_sum(float v) {
  v += dpp<0x128>(v); v += dpp<0x124>(v); v += dpp<0x122>(v); v += dpp<0x121>(v);
  return v;
}
DI float wave_sum(float v) {
  v = row16_sum(v);
  int x = __builtin_bit_cast(int, v);
  float a = __builtin_bit_cast(float, __builtin_amdgcn_readlane(x, 0));
  float b = __builtin_bit_cast(float, __builtin_amdgcn_readlane(x, 16));
  float c = __builtin_bit_cast(float, __builtin_amdgcn_readlane(x, 32));
  float d = __builtin_bit_cast(float, __builtin_amdgcn_readlane(x, 48));
  return (a + b) + (c + d);
}
DI u32x4 cmul_pk(f4 re, f4 im, f4 tx, f4 ty) {
  f4 a = re * tx - im * ty, b = re * ty + im * tx;
  return u32x4{pk_h(a[0], b[0]), pk_h(a[1], b[1]), pk_h(a[2], b[2]), pk_h(a[3], b[3])};
}
DI float opq(float v) { asm("" : "+v"(v)); return v; }
DI float h_lo(u32 v) { return (float)__builtin_bit_cast(h2, v)[0]; }
DI float h_hi(u32 v) { return (float)__builtin_bit_cast(h2, v)[1]; }
template <int SG> DI void radix8_half(int e, f4 ar, f4 ai, f4 br, f4 bi, f4& xr, f4& xi) {
  f4 pr, pi;
  if (e == 0) {
    pr = ar + br; pi = ai + bi;
  } else {
    const float r2 = 0.70710678118654752f;
    const f4 cx = {1.f, r2, 0.f, -r2}, cy = {0.f, SG * r2, SG * 1.f, SG * r2};
    f4 dr = ar - br, di = ai - bi;
    pr = dr * cx - di * cy; pi = dr * cy + di * cx;
  }
  f2 sr = pr.xy + pr.zw, si = pi.xy + pi.zw;
  f2 dr = pr.xy - pr.zw, di = pi.xy - pi.zw;
  xr[0] = opq(sr.x + sr.y); xi[0] = opq(si.x + si.y);
  xr[2] = opq(sr.x - sr.y); xi[2] = opq(si.x - si.y);
  if (SG < 0) {
    xr[1] = opq(dr.x + di.y); xi[1] = opq(di.x - dr.y);
    xr[3] = opq(dr.x - di.y); xi[3] = opq(di.x + dr.y);
  } else {
    xr[1] = opq(dr.x - di.y); xi[1] = opq(di.x + dr.y);
    xr[3] = opq(dr.x + di.y); xi[3] = opq(di.x - dr.y);
  }
}
DI f4 mfma_h(u32x4 a, u32x4 b, f4 c) {
  return __builtin_amdgcn_mfma_f32_16x16x32_f16(__builtin_bit_cast(h8, a), __builtin_bit_cast(h8, b), c, 0, 0, 0);
}
DI f4 mfma_b(u32x4 a, u32x4 b, f4 c) {
  return __builtin_amdgcn_mfma_f32_16x16x32_bf16(__builtin_bit_cast(b8, a), __builtin_bit_cast(b8, b), c, 0, 0, 0);
}
DI u32x2 tr_read(const char* p) {
  return __builtin_bit_cast(u32x2, __builtin_amdgcn_ds_read_tr16_b64_v4i16((LDS_AS s4*)p));
}
DI void glds16(const void* g, char* lds_wave_base) {
  __builtin_amdgcn_global_load_lds((const GLB_AS u32*)g, (LDS_AS u32*)lds_wave_base, 16, 0, 0);
}
template <int N> DI void wait_vm() { asm volatile("s_waitcnt vmcnt(%0)" ::"n"(N) : "memory"); }
DI void wait_lgkm0() { asm volatile("s_waitcnt lgkmcnt(0)" ::: "memory"); }
DI void raw_barrier() { __builtin_amdgcn_s_barrier(); }
DI f4 sel4(bool c, f4 a, f4 b) { return f4{c ? a[0] : b[0], c ? a[1] : b[1], c ? a[2] : b[2], c ? a[3] : b[3]}; }
DI int uni(int v) { return __builtin_amdgcn_readfirstlane(v); }

DI f2 ln_scalars(float mean, float var) {
  float m = rbf(mean);
  float v = rbf(var);
  float den = rbf(__builtin_sqrtf(rbf(v + 1e-5f)));
  return f2{m, 1.0f / den};
}
DI float gelu_f(float u) {
  const float c1 = -2.0f * 0.7978845608028654f * 1.4426950408889634f;
  const float c2 = c1 * 0.044715f;
  float e = __builtin_amdgcn_exp2f(u * (c1 + c2 * u * u));
  return u * __builtin_amdgcn_rcpf(1.0f + e);
}
DI f2 w_scales(const u32* wmax, int lane) {
  u32 m = wmax[lane];
  for (int o = 32; o > 0; o >>= 1) { u32 t = __shfl_xor(m, o); m = m > t ? m : t; }
  int e = (int)(m >> 7);
  e = e < 4 ? 4 : (e > 254 ? 254 : e);
  return f2{__builtin_bit_cast(float, (u32)(256 - e) << 23), __builtin_bit_cast(float, (u32)(e - 2) << 23)};
}

constexpr int K1_TAB = 0, K1_XN = 12288, K1_STG = 24576, K1_LDS = 41216;
__global__ __launch_bounds__(512) void k1_fwd(const u32* __restrict__ x, const u32x4* __restrict__ wr,
                                              const u32x4* __restrict__ wi, char* __restrict__ ws) {
  extern __shared__ __attribute__((aligned(16))) char sm[];
  const int tid = threadIdx.x, lane = tid & 63, w = uni(tid >> 6);
  const int n = lane & 15, g = lane >> 4;
  STAMP_DECL; STAMP(0);
  if (blockIdx.x >= 256) {
    const int q = blockIdx.x - 256;
    const int jbase = 8 * (q & 15) + 512 * (q >> 4);
    u32 mx = 0;
    for (int c = tid; c < 8 * 321; c += 512) {
      int part = c / 321, off = c - part * 321;
      const u32x4* src = (part >> 2) ? wi : wr;
      u32x4 v = src[(size_t)((jbase + 128 * (part & 3)) >> 3) * 321 + off];
      *(u32x4*)(sm + c * 16) = v;
#pragma unroll
      for (int i = 0; i < 4; ++i) {
        u32 a = v[i] & 0x7fffu, b = (v[i] >> 16) & 0x7fffu;
        mx = mx > a ? mx : a;
        mx = mx > b ? mx : b;
      }
    }
    for (int o = 32; o > 0; o >>= 1) { u32 t = __shfl_xor(mx, o); mx = mx > t ? mx : t; }
    u32* red = (u32*)(sm + 41088);
    if (lane == 0) red[w] = mx;
    STAMP(1);
    __syncthreads();
    STAMP(2);
    if (tid == 0) {
      u32 m = 0;
      for (int i = 0; i < 8; ++i) m = m > red[i] ? m : red[i];
      ((u32*)(ws + WS_WMAX))[q] = m;
    }
    const u16* s16 = (const u16*)sm;
    u32x4* dst = (u32x4*)(ws + WS_WT);
    for (int c = tid; c < 8 * 321; c += 512) {
      int k = c >> 3, ww = c & 7;
      u32x4 o;
#pragma unroll
      for (int r = 0; r < 4; ++r)
        o[r] = (u32)s16[r * 2568 + ww * 321 + k] | ((u32)s16[(4 + r) * 2568 + ww * 321 + k] << 16);
      dst[(size_t)k * 512 + q * 8 + ww] = o;
    }
    STAMP_OUT(0);
    return;
  }
  const int b = blockIdx.x;
  const int rg = (b & 7) * 32 + (b >> 3);
  const int s = rg * 8 + w;
  u32 xv[5];
#pragma unroll
  for (int i = 0; i < 5; ++i) xv[i] = x[(size_t)s * 320 + i * 64 + lane];
  glds16(&g_tab_fwd.v[w][lane][0], sm + K1_TAB + w * 1024);
  if (w < 4) glds16(&g_tab_fwd.v[8 + w][lane][0], sm + K1_TAB + (8 + w) * 1024);
  STAMP(1);
  wait_vm<0>();
  STAMP(2);
  f2 v[5];
#pragma unroll
  for (int i = 0; i < 5; ++i) v[i] = f2{bf_lo(xv[i]), bf_hi(xv[i])};
  const f2 s2 = ((v[0] + v[1]) + (v[2] + v[3])) + v[4];
  const float mean = wave_sum(opq(s2.x) + opq(s2.y)) * (1.0f / 640.0f);
  f2 q2 = {0.f, 0.f};
#pragma unroll
  for (int i = 0; i < 5; ++i) { f2 d = v[i] - mean; q2 += d * d; }
  const float var = wave_sum(opq(q2.x) + opq(q2.y)) * (1.0f / 640.0f);
  const f2 ln = ln_scalars(mean, var);
  char* xn = sm + K1_XN + w * 1536;
#pragma unroll
  for (int i = 0; i < 5; ++i) {
    u32 d = pk_bf(v[i].x - ln.x, v[i].y - ln.x);
    u32 o = pk_bf(bf_lo(d) * ln.y, bf_hi(d) * ln.y);
    *(u32*)(xn + (i * 64 + lane) * 4) = pk_h(bf_lo(o), bf_hi(o));
  }
  *(u32*)(xn + (320 + lane) * 4) = 0u;
  STAMP(3);
  __syncthreads();
  STAMP(4);
  const u32x4* tab = (const u32x4*)(sm + K1_TAB) + lane;
  u32x2 a0 = tr_read(xn + lane * 8), a1 = tr_read(xn + 512 + lane * 8), a2 = tr_read(xn + 1024 + lane * 8);
  u32x4 af = {a0[0], a0[1], a1[0], a1[1]};
  u32x4 tre = tab[4 * 64], tim = tab[5 * 64], fr = tab[10 * 64], fi = tab[11 * 64];
  u32* stg = (u32*)(sm + K1_STG) + w * 328;
#pragma unroll
  for (int nt = 0; nt < 2; ++nt) {
    f4 z = {0.f, 0.f, 0.f, 0.f};
    f4 re = mfma_h(af, tab[(0 + nt) * 64], z);
    f4 im = mfma_h(af, tab[(2 + nt) * 64], z);
    const u32x4 at = nt ? u32x4{0u, 0u, a2[0], a2[1]} : u32x4{a2[0], a2[1], 0u, 0u};
    re = mfma_h(at, tre, re);
    im = mfma_h(at, tim, im);
    u32x4 fb_ = cmul_pk(re, im, __builtin_bit_cast(f4, tab[(6 + 2 * nt) * 64]), __builtin_bit_cast(f4, tab[(7 + 2 * nt) * 64]));
    f4 ore = mfma_h(fr, fb_, z), oim = mfma_h(fi, fb_, z);
    const int k1 = 16 * nt + n;
#pragma unroll
    for (int r = 0; r < 4; ++r) {
      int k2 = 4 * g + r, k = k1 + 40 * k2;
      int kt = k2 < 8 ? k : 640 - k;
      bool ok = k1 <= 20 && !(k1 == 0 && k2 > 8) && !(k1 == 20 && k2 >= 8);
      if (ok) stg[kt] = pk_h(ore[r], oim[r]);
    }
  }
  STAMP(5);
  __syncthreads();
  STAMP(6);
  if (tid < KT) {
    const u32* sp = (const u32*)(sm + K1_STG) + tid;
    u32x4 o0 = {sp[0], sp[328], sp[2 * 328], sp[3 * 328]};
    u32x4 o1 = {sp[4 * 328], sp[5 * 328], sp[6 * 328], sp[7 * 328]};
    u32x4* dst = (u32x4*)(ws + WS_T1) + (size_t)tid * 512 + rg * 2;
    dst[0] = o0;
    dst[1] = o1;
  }
  STAMP(7);
  STAMP_OUT(0);
}

constexpr int K2_LDS = 10240 + 16384;
constexpr int K2_NWIN = 68, K2_NWOUT = 34, K2_NSTAT = 8;
DI void transpose16(const u32* __restrict__ src, u32x4* __restrict__ dst, int item, int npair, int nitems) {
  if (item >= nitems) return;
  int kb = item / npair, p = item - kb * npair;
  u32 v[16];
#pragma unroll
  for (int i = 0; i < 16; ++i) v[i] = src[(size_t)(kb * 16 + i) * npair + p];
  u32x4 o[4];
#pragma unroll
  for (int i = 0; i < 8; ++i) {
    o[i >> 2][i & 3] = __builtin_amdgcn_perm(v[2 * i + 1], v[2 * i], 0x05040100u);
    o[2 + (i >> 2)][i & 3] = __builtin_amdgcn_perm(v[2 * i + 1], v[2 * i], 0x07060302u);
  }
  u32x4* d = dst + (size_t)item * 4;
  d[0] = o[0]; d[1] = o[1]; d[2] = o[2]; d[3] = o[3];
}
__global__ __launch_bounds__(512) void k2_col(const u32* __restrict__ w_in, const u32* __restrict__ w_out,
                                              char* __restrict__ ws) {
  extern __shared__ __attribute__((aligned(16))) char sm[];
  const int tid = threadIdx.x, lane = tid & 63, w = uni(tid >> 6);
  const int n = lane & 15, g = lane >> 4;
  STAMP_DECL; STAMP(0);
  if (blockIdx.x >= KT) {
    int q = blockIdx.x - KT;
    if (q < K2_NWIN) {
      transpose16(w_in, (u32x4*)(ws + WS_WIN), q * 512 + tid, F / 2, 40 * (F / 2));
    } else if (q < K2_NWIN + K2_NWOUT) {
      transpose16(w_out, (u32x4*)(ws + WS_WOUT), (q - K2_NWIN) * 512 + tid, D / 2, 54 * (D / 2));
    } else {
      u32x4* st = (u32x4*)(ws + WS_STAT) + (q - K2_NWIN - K2_NWOUT) * 2048 + tid;
      u32x4 f = {~0u, ~0u, ~0u, ~0u};
      st[0] = f; st[512] = f; st[1024] = f; st[1536] = f;
    }
    STAMP_OUT(1);
    return;
  }
  const int kt = blockIdx.x;
  const int bp = tid & 255, e = w >> 2;
  const u32* t1 = (const u32*)(ws + WS_T1) + (size_t)kt * 2048 + bp;
  u32 uv[8];
#pragma unroll
  for (int a = 0; a < 8; ++a) uv[a] = t1[a * 256];
  const u32x4 wv = *((const u32x4*)(ws + WS_WT) + (size_t)kt * 512 + lane * 8 + w);
  const f2 wsc = w_scales((const u32*)(ws + WS_WMAX), lane);
  const float rs8 = 0.35355339059327376f;
  f4 tax, tay, t1x, t1y, t3x, t3y;
  u32x4 Fr, Fi, Gr, Gi;
#pragma unroll
  for (int i = 0; i < 4; ++i) {
    float a0 = (float)(bp * (2 * i + e)) * (1.0f / 2048.0f);
    float a1 = (float)((4 * g + i) * n) * (1.0f / 256.0f);
    float a3 = (float)((16 * (4 * g + i) + n) * w) * (1.0f / 2048.0f);
    float a4 = (float)(((4 * g + i) * n) & 15) * (1.0f / 16.0f);
    tax[i] = __builtin_amdgcn_cosf(a0); tay[i] = __builtin_amdgcn_sinf(a0);
    t1x[i] = __builtin_amdgcn_cosf(a1); t1y[i] = __builtin_amdgcn_sinf(a1);
    t3x[i] = __builtin_amdgcn_cosf(a3); t3y[i] = __builtin_amdgcn_sinf(a3);
    float c = __builtin_amdgcn_cosf(a4), sn_ = __builtin_amdgcn_sinf(a4);
    Fr[i] = pk_h(c, sn_); Fi[i] = pk_h(-sn_, c);
    Gr[i] = pk_h(c, -sn_); Gi[i] = pk_h(sn_, c);
  }
  STAMP(1);
  wait_vm<0>();
  STAMP(2);
  tax *= rs8; tay *= -rs8; t1x *= 0.25f; t1y *= 0.25f; t3x *= 0.25f * rs8; t3y *= 0.25f * rs8;
  {
    f4 ar, ai, br, bi, xr, xi;
#pragma unroll
    for (int a = 0; a < 4; ++a) { ar[a] = h_lo(uv[a]); ai[a] = h_hi(uv[a]); br[a] = h_lo(uv[a + 4]); bi[a] = h_hi(uv[a + 4]); }
    radix8_half<-1>(e, ar, ai, br, bi, xr, xi);
    u32x4 o = cmul_pk(xr, xi, tax, tay);
    u32* ex = (u32*)sm + (bp & 15) * 20 + (bp >> 4) + e * 320;
#pragma unroll
    for (int m = 0; m < 4; ++m) ex[m * 640] = o[m];
  }
  STAMP(3);
  __syncthreads();
  STAMP(4);
  const f4 z = {0.f, 0.f, 0.f, 0.f};
  u32x4 fa = *(const u32x4*)(sm + (w * 320 + n * 20 + 4 * g) * 4);
  f4 re = mfma_h(fa, Fr, z), im = mfma_h(fa, Fi, z);
  fa = cmul_pk(re, im, t1x, -t1y);
  re = mfma_h(Fr, fa, z); im = mfma_h(Fi, fa, z);
  {
    const float wk = 0.25f * wsc.x;
    f4 wx, wy;
#pragma unroll
    for (int r = 0; r < 4; ++r) { wx[r] = bf_lo(wv[r]); wy[r] = bf_hi(wv[r]); }
    fa = cmul_pk(re, im, wx * wk, wy * wk);
  }
  re = mfma_h(fa, Gr, z); im = mfma_h(fa, Gi, z);
  fa = cmul_pk(re, im, t1x, t1y);
  re = mfma_h(Gr, fa, z); im = mfma_h(Gi, fa, z);
  {
    float* ex = (float*)(sm + 10240) + w * 256 + 64 * g + n;
    f4 a = re * t3x - im * t3y, b = re * t3y + im * t3x;
#pragma unroll
    for (int r = 0; r < 4; ++r) { ex[16 * r] = a[r]; ex[2048 + 16 * r] = b[r]; }
  }
  STAMP(5);
  __syncthreads();
  STAMP(6);
  {
    const float* ex = (const float*)(sm + 10240) + bp;
    f4 ar, ai, br, bi, xr, xi;
#pragma unroll
    for (int j = 0; j < 4; ++j) {
      ar[j] = ex[j * 256]; br[j] = ex[(j + 4) * 256];
      ai[j] = ex[2048 + j * 256]; bi[j] = ex[2048 + (j + 4) * 256];
    }
    radix8_half<1>(e, ar, ai, br, bi, xr, xi);
    u32* t2p = (u32*)(ws + WS_T2) + (size_t)kt * 2048 + bp + e * 256;
#pragma unroll
    for (int m = 0; m < 4; ++m) t2p[m * 512] = pk_h(xr[m], xi[m]);
  }
  STAMP(7);
  STAMP_OUT(1);
}

constexpr int K3_TAB = 0, K3_Z = 12288, K3_ZSTR = 340, K3_LDS = 12288 + 8 * K3_ZSTR * 4;
__global__ __launch_bounds__(512) void k3_inv(const u32x2* __restrict__ x, char* __restrict__ ws) {
  extern __shared__ __attribute__((aligned(16))) char sm[];
  const int tid = threadIdx.x, lane = tid & 63, w = uni(tid >> 6);
  const int n = lane & 15, g = lane >> 4;
  const int b = blockIdx.x;
  const int rg = (b & 7) * 32 + (b >> 3);
  const int s = rg * 8 + w;
  STAMP_DECL; STAMP(0);
  u32x4 z0, z1;
  if (tid < 336) {
    int k1 = tid >> 4, k2 = tid & 15, k = k1 + 40 * k2;
    int kt = k2 < 8 ? k : 640 - k;
    const u32x4* src = (const u32x4*)(ws + WS_T2) + (size_t)kt * 512 + rg * 2;
    z0 = src[0]; z1 = src[1];
  }
  u32x2 xv[3];
#pragma unroll
  for (int nt = 0; nt < 3; ++nt) {
    int a = 16 * nt + n;
    a = a < 40 ? a : 39;
    xv[nt] = x[(size_t)s * 160 + a * 4 + g];
  }
  const f2 wsc = w_scales((const u32*)(ws + WS_WMAX), lane);
  glds16(&g_tab_inv.v[w][lane][0], sm + K3_TAB + w * 1024);
  if (w < 4) glds16(&g_tab_inv.v[8 + w][lane][0], sm + K3_TAB + (8 + w) * 1024);
  STAMP(1);
  wait_vm<0>();
  STAMP(2);
  if (tid < 336) {
    u32* zp = (u32*)(sm + K3_Z) + tid;
#pragma unroll
    for (int i = 0; i < 4; ++i) { zp[i * K3_ZSTR] = z0[i]; zp[(4 + i) * K3_ZSTR] = z1[i]; }
  }
  STAMP(3);
  __syncthreads();
  STAMP(4);
  const u32x4* tab = (const u32x4*)(sm + K3_TAB) + lane;
  const f4 z = {0.f, 0.f, 0.f, 0.f};
  u32x4 gr = tab[0], gi = tab[64];
  u32x4 fv[2];
#pragma unroll
  for (int mt = 0; mt < 2; ++mt) {
    int k1 = 16 * mt + n;
    k1 = k1 <= 20 ? k1 : 20;
    u32x4 fa = *(const u32x4*)(sm + K3_Z + (w * K3_ZSTR + k1 * 16 + 4 * g) * 4);
    f4 re = mfma_h(fa, gr, z), im = mfma_h(fa, gi, z);
    fv[mt] = cmul_pk(re, im, __builtin_bit_cast(f4, tab[(2 + 2 * mt) * 64]), __builtin_bit_cast(f4, tab[(3 + 2 * mt) * 64]));
  }
  float hv[12];
  u32x2 hp[3];
  float sum = 0.f;
  const bool v2 = n < 8;
#pragma unroll
  for (int nt = 0; nt < 3; ++nt) {
    f4 y = mfma_h(fv[0], tab[(6 + 2 * nt) * 64], z);
    y = mfma_h(fv[1], tab[(7 + 2 * nt) * 64], y);
    u32 y0 = pk_bf(y[0] * wsc.y, y[1] * wsc.y), y1 = pk_bf(y[2] * wsc.y, y[3] * wsc.y);
    hp[nt][0] = pk_bf(bf_lo(xv[nt][0]) + bf_lo(y0), bf_hi(xv[nt][0]) + bf_hi(y0));
    hp[nt][1] = pk_bf(bf_lo(xv[nt][1]) + bf_lo(y1), bf_hi(xv[nt][1]) + bf_hi(y1));
    hv[4 * nt + 0] = bf_lo(hp[nt][0]); hv[4 * nt + 1] = bf_hi(hp[nt][0]);
    hv[4 * nt + 2] = bf_lo(hp[nt][1]); hv[4 * nt + 3] = bf_hi(hp[nt][1]);
    float t = (hv[4 * nt] + hv[4 * nt + 1]) + (hv[4 * nt + 2] + hv[4 * nt + 3]);
    sum += (nt < 2 || v2) ? t : 0.f;
  }
  const float mean = wave_sum(sum) * (1.0f / 640.0f);
  float sq = 0.f;
#pragma unroll
  for (int nt = 0; nt < 3; ++nt) {
    float t = 0.f;
#pragma unroll
    for (int r = 0; r < 4; ++r) { float d = hv[4 * nt + r] - mean; t += d * d; }
    sq += (nt < 2 || v2) ? t : 0.f;
  }
  const float var = wave_sum(sq) * (1.0f / 640.0f);
  const f2 ln = ln_scalars(mean, var);
  STAMP(5);
  u32x2* hb = (u32x2*)(ws + WS_H);
  u32x2* hn = (u32x2*)(ws + WS_HN);
#pragma unroll
  for (int nt = 0; nt < 3; ++nt) {
    if (nt < 2 || v2) {
      u32 d0 = pk_bf(hv[4 * nt] - ln.x, hv[4 * nt + 1] - ln.x), d1 = pk_bf(hv[4 * nt + 2] - ln.x, hv[4 * nt + 3] - ln.x);
      u32x2 o = {pk_bf(bf_lo(d0) * ln.y, bf_hi(d0) * ln.y), pk_bf(bf_lo(d1) * ln.y, bf_hi(d1) * ln.y)};
      size_t idx = ((size_t)(16 * nt + n) * 2048 + s) * 4 + g;
      hb[idx] = hp[nt];
      hn[idx] = o;
    }
  }
  STAMP(7);
  STAMP_OUT(2);
}

DI void tile_map(int& rt, int& cg) {
  int b = blockIdx.x, x = b & 7, i = b >> 3;
  rt = (x >> 1) * 8 + (i >> 2);
  cg = (x & 1) * 4 + (i & 3);
}
DI int cg_start(int cg) { return cg * 7 - (cg >> 2); }
DI int cg_count(int cg) { return (cg & 3) == 3 ? 6 : 7; }

template <int I, int N, class Fn> DI void static_for(Fn&& f) {
  if constexpr (I < N) { f(std::integral_constant<int, I>{}); static_for<I + 1, N>(f); }
}

constexpr int G1_NS = 8, G1_STG = 14336, G1_RED = G1_NS * G1_STG, G1_LDS = G1_RED + 32768;
__global__ __launch_bounds__(512, 2) void k4_gemm1(char* __restrict__ ws) {
  extern __shared__ __attribute__((aligned(16))) char sm[];
  const int tid = threadIdx.x, lane = tid & 63, w = uni(tid >> 6);
  const int n = lane & 15, g = lane >> 4;
  int rt, cg;
  tile_map(rt, cg);
  const int nt_all = cg_count(cg), t0 = cg_start(cg);
  const int kg = w >> 2, mh = (w >> 1) & 1, np = (w ^ (w >> 2)) & 1;
  const int nfirst = np ? nt_all - 3 : 0, ncnt = np ? 3 : nt_all - 3;
  const int rows = nt_all * 16, per = rows >> 1;
  STAMP_DECL; STAMP(0);
  const char* bsrc[2];
#pragma unroll
  for (int i = 0; i < 2; ++i) {
    int c = (2 * w + i) * per + (lane < per ? lane : 0);
    int kb = c / (2 * rows), rem = c - kb * 2 * rows;
    bsrc[i] = ws + WS_WIN + ((size_t)kb * F + t0 * 16) * 32 + rem * 16;
  }
  auto issue = [&](int st, int slot) {
    size_t off = st < 10 ? (size_t)st * (4 * F * 32) + FH * 32 : (size_t)(st - 10) * (4 * F * 32);
    if (lane < per) {
      glds16(bsrc[0] + off, sm + slot * G1_STG + (2 * w) * per * 16);
      glds16(bsrc[1] + off, sm + slot * G1_STG + (2 * w + 1) * per * 16);
    }
  };
  u32x4 af[10][2];
  {
    const char* ap = ws + WS_HN + ((size_t)(2 * kg + (g >> 1)) * 2048 + rt * 64 + mh * 32 + n) * 32 + (g & 1) * 16;
#pragma unroll
    for (int st = 0; st < 10; ++st)
#pragma unroll
      for (int m = 0; m < 2; ++m) af[st][m] = *(const u32x4*)(ap + (size_t)st * (4 * 2048 * 32) + m * 512);
  }
#pragma unroll
  for (int st = 0; st < G1_NS - 1; ++st) issue(st, st);
  const int boff = (2 * kg + (g >> 1)) * rows * 32 + (nfirst * 16 + n) * 32 + (g & 1) * 16;
  f4 acc[4][2];
  u32 z2p[4][2];
  const int srow = rt * 64 + (2 * mh + kg) * 16;
  auto reduce = [&]() {
    f4* red = (f4*)(sm + G1_RED);
#pragma unroll
    for (int j = 0; j < 4; ++j) red[(w * 4 + j) * 64 + lane] = sel4(kg, acc[j][0], acc[j][1]);
    wait_lgkm0(); raw_barrier();
    const int pw = (w ^ 4) ^ 1;
#pragma unroll
    for (int j = 0; j < 4; ++j) acc[j][0] = sel4(kg, acc[j][1], acc[j][0]) + red[(pw * 4 + j) * 64 + lane];
  };
  static_for<0, 20>([&](auto ST) {
    constexpr int st = ST.value;
    constexpr int left = (19 - st) < (G1_NS - 2) ? (19 - st) : (G1_NS - 2);
    if constexpr (st == 0 || st == 10) {
#pragma unroll
      for (int j = 0; j < 4; ++j) { acc[j][0] = f4{0.f, 0.f, 0.f, 0.f}; acc[j][1] = f4{0.f, 0.f, 0.f, 0.f}; }
    }
    if constexpr (st == 0) STAMP(1);
    wait_vm<2 * left>(); wait_lgkm0(); raw_barrier();
    if constexpr (st == 0) STAMP(2);
    if constexpr (st == 1) STAMP(3);
    if constexpr (st == 10) STAMP(6);
    if constexpr (st + G1_NS - 1 < 20) issue(st + G1_NS - 1, (st + G1_NS - 1) % G1_NS);
    const char* bp = sm + (st % G1_NS) * G1_STG + boff;
#pragma unroll
    for (int j = 0; j < 4; ++j) {
      if (j < ncnt) {
        u32x4 bf = *(const u32x4*)(bp + j * 512);
        if constexpr (st < 10) {
          acc[j][0] = mfma_b(af[st][0], bf, acc[j][0]);
          acc[j][1] = mfma_b(af[st][1], bf, acc[j][1]);
        } else {
          acc[j][0] = mfma_b(bf, af[st - 10][0], acc[j][0]);
          acc[j][1] = mfma_b(bf, af[st - 10][1], acc[j][1]);
        }
      }
    }
    if constexpr (st == 9) {
      STAMP(4);
      reduce();
      STAMP(5);
      float zv[4][4], sum[4] = {0.f, 0.f, 0.f, 0.f};
#pragma unroll
      for (int j = 0; j < 4; ++j) {
        u32 u0 = pk_bf(acc[j][0][0], acc[j][0][1]), u1 = pk_bf(acc[j][0][2], acc[j][0][3]);
        z2p[j][0] = pk_bf(gelu_f(bf_lo(u0)), gelu_f(bf_hi(u0)));
        z2p[j][1] = pk_bf(gelu_f(bf_lo(u1)), gelu_f(bf_hi(u1)));
        zv[j][0] = bf_lo(z2p[j][0]); zv[j][1] = bf_hi(z2p[j][0]);
        zv[j][2] = bf_lo(z2p[j][1]); zv[j][3] = bf_hi(z2p[j][1]);
#pragma unroll
        for (int r = 0; r < 4; ++r) sum[r] += (j < ncnt) ? zv[j][r] : 0.f;
      }
      const float inv = 1.0f / (float)(ncnt * 16);
      float mean[4], m2[4];
#pragma unroll
      for (int r = 0; r < 4; ++r) {
        mean[r] = row16_sum(sum[r]) * inv;
        float q = 0.f;
#pragma unroll
        for (int j = 0; j < 4; ++j) { float d = zv[j][r] - mean[r]; q += (j < ncnt) ? d * d : 0.f; }
        m2[r] = row16_sum(q);
      }
      float mm = n == 0 ? mean[0] : n == 1 ? mean[1] : n == 2 ? mean[2] : mean[3];
      float qq = n == 0 ? m2[0] : n == 1 ? m2[1] : n == 2 ? m2[2] : m2[3];
      if (n < 4) {
        u64 gr = (u64)__builtin_bit_cast(u32, mm) | ((u64)(__builtin_bit_cast(u32, qq) & 0x7fffffffu) << 32);
        __hip_atomic_store((GLB_AS u64*)(ws + WS_STAT) + (size_t)(srow + 4 * g + n) * 16 + cg * 2 + np, gr,
                           __ATOMIC_RELAXED, __HIP_MEMORY_SCOPE_AGENT);
      }
    }
  });
  STAMP(7);
  wait_lgkm0(); raw_barrier();
  reduce();
  STAMP(8);
  {
    u32x2* z1 = (u32x2*)(ws + WS_Z1);
#pragma unroll
    for (int j = 0; j < 4; ++j) {
      if (j < ncnt) {
        u32 u0 = pk_bf(acc[j][0][0], acc[j][0][1]), u1 = pk_bf(acc[j][0][2], acc[j][0][3]);
        u32x2 o = {pk_bf(gelu_f(bf_lo(u0)), gelu_f(bf_hi(u0))), pk_bf(gelu_f(bf_lo(u1)), gelu_f(bf_hi(u1)))};
        z1[((size_t)(t0 + nfirst + j) * 2048 + srow + n) * 4 + g] = o;
      }
    }
  }
  {
    const GLB_AS u64* sp = (const GLB_AS u64*)(ws + WS_STAT) + (size_t)(srow + 4 * g) * 16 + n;
    u64 gr[4];
    for (int spin = 0; spin < (1 << 22); ++spin) {
      bool ok = true;
#pragma unroll
      for (int r = 0; r < 4; ++r) {
        gr[r] = __hip_atomic_load(sp + r * 16, __ATOMIC_RELAXED, __HIP_MEMORY_SCOPE_AGENT);
        ok = ok && !(gr[r] >> 63);
      }
      if (__all(ok)) break;
      __builtin_amdgcn_s_sleep(1);
    }
    STAMP(9);
    const float cnt = (n & 1) ? 48.f : (((n >> 1) & 3) == 3 ? 48.f : 64.f);
    f2 ln[4];
#pragma unroll
    for (int r = 0; r < 4; ++r) {
      float mp = __builtin_bit_cast(float, (u32)gr[r]), qp = __builtin_bit_cast(float, (u32)(gr[r] >> 32));
      float mean = row16_sum(mp * cnt) * (1.0f / 864.0f);
      float d = mp - mean;
      float var = row16_sum(qp + cnt * d * d) * (1.0f / 864.0f);
      ln[r] = ln_scalars(mean, var);
    }
    u32x2* z2 = (u32x2*)(ws + WS_Z2);
#pragma unroll
    for (int j = 0; j < 4; ++j) {
      if (j < ncnt) {
        u32 d0 = pk_bf(bf_lo(z2p[j][0]) - ln[0].x, bf_hi(z2p[j][0]) - ln[1].x);
        u32 d1 = pk_bf(bf_lo(z2p[j][1]) - ln[2].x, bf_hi(z2p[j][1]) - ln[3].x);
        u32x2 o = {pk_bf(bf_lo(d0) * ln[0].y, bf_hi(d0) * ln[1].y), pk_bf(bf_lo(d1) * ln[2].y, bf_hi(d1) * ln[3].y)};
        z2[((size_t)(srow >> 4) * FH + (t0 + nfirst + j) * 16 + n) * 4 + g] = o;
      }
    }
  }
  STAMP(10);
  STAMP_OUT(3);
}

constexpr int G2_NS = 7, G2_A = 14336, G2_STG = 14336 + 8192, G2_LDS = G2_NS * G2_STG;
__global__ __launch_bounds__(512, 2) void k5_gemm2(const char* __restrict__ wsp, const u16* __restrict__ bsp,
                                                   char* __restrict__ ws) {
  extern __shared__ __attribute__((aligned(16))) char sm[];
  const int tid = threadIdx.x, lane = tid & 63, w = uni(tid >> 6);
  const int n = lane & 15, g = lane >> 4;
  int rt, cg;
  tile_map(rt, cg);
  const int nt_all = cg_count(cg), t0 = cg_start(cg);
  const int kg = w >> 2, mh = (w >> 1) & 1, np = (w ^ (w >> 2)) & 1;
  const int nfirst = np ? nt_all - 3 : 0, ncnt = np ? 3 : nt_all - 3;
  const int rows = nt_all * 16, per = rows >> 1;
  STAMP_DECL; STAMP(0);
  const char* asrc[2];
#pragma unroll
  for (int i = 0; i < 2; ++i) {
    int c = (2 * w + i) * per + (lane < per ? lane : 0);
    int kb = c / (2 * rows), rem = c - kb * 2 * rows;
    asrc[i] = ws + WS_Z2 + ((size_t)kb * FH + t0 * 16) * 32 + rem * 16;
  }
  const int brow = w * 8 + (lane >> 3);
  const char* bsrc = wsp + (size_t)(rt * 64 + brow) * 4096 + (((lane & 7) ^ ((brow >> 1) & 7)) << 4);
  const int trow = rt * 64 + (2 * mh + kg) * 16 + n;
  u32x2 z1v[4];
#pragma unroll
  for (int j = 0; j < 4; ++j) {
    int jj = j < ncnt ? j : 0;
    z1v[j] = ((const u32x2*)(ws + WS_Z1))[((size_t)(t0 + nfirst + jj) * 2048 + trow) * 4 + g];
  }
  const float bias = bf_lo(bsp[trow]);
  auto issue = [&](int st, int slot) {
    char* base = sm + slot * G2_STG;
    glds16(bsrc + st * 128, base + G2_A + w * 1024);
    if (lane < per) {
      glds16(asrc[0] + (size_t)st * (4 * FH * 32), base + (2 * w) * per * 16);
      glds16(asrc[1] + (size_t)st * (4 * FH * 32), base + (2 * w + 1) * per * 16);
    }
  };
#pragma unroll
  for (int st = 0; st < G2_NS - 1; ++st) issue(st, st);
  const int aoff = (2 * kg + (g >> 1)) * rows * 32 + (nfirst * 16 + n) * 32 + (g & 1) * 16;
  int boff[2];
#pragma unroll
  for (int m = 0; m < 2; ++m) {
    int r = mh * 32 + m * 16 + n;
    boff[m] = G2_A + r * 128 + (((4 * kg + g) ^ ((r >> 1) & 7)) << 4);
  }
  f4 acc[4][2];
#pragma unroll
  for (int j = 0; j < 4; ++j) { acc[j][0] = f4{0.f, 0.f, 0.f, 0.f}; acc[j][1] = f4{0.f, 0.f, 0.f, 0.f}; }
  auto comp = [&](int slot) {
    const char* base = sm + slot * G2_STG;
    u32x4 b0 = *(const u32x4*)(base + boff[0]), b1 = *(const u32x4*)(base + boff[1]);
#pragma unroll
    for (int j = 0; j < 4; ++j) {
      if (j < ncnt) {
        u32x4 a = *(const u32x4*)(base + aoff + j * 512);
        acc[j][0] = mfma_b(a, b0, acc[j][0]);
        acc[j][1] = mfma_b(a, b1, acc[j][1]);
      }
    }
  };
  int cur = 0, nxt = G2_NS - 1;
  STAMP(1);
  for (int st = 0; st < 32 - (G2_NS - 1); ++st) {
    wait_vm<3 * (G2_NS - 2)>(); wait_lgkm0(); raw_barrier();
    issue(st + G2_NS - 1, nxt);
    comp(cur);
    cur = cur + 1 == G2_NS ? 0 : cur + 1;
    nxt = nxt + 1 == G2_NS ? 0 : nxt + 1;
  }
  static_for<0, G2_NS - 1>([&](auto I) {
    wait_vm<3 * (G2_NS - 2 - I.value)>(); wait_lgkm0(); raw_barrier();
    comp(cur);
    cur = cur + 1 == G2_NS ? 0 : cur + 1;
  });
  STAMP(6);
  wait_lgkm0(); raw_barrier();
  f4* red = (f4*)sm;
#pragma unroll
  for (int j = 0; j < 4; ++j) red[(w * 4 + j) * 64 + lane] = sel4(kg, acc[j][0], acc[j][1]);
  wait_lgkm0(); raw_barrier();
  const int pw = (w ^ 4) ^ 1;
  u32x2* P = (u32x2*)(ws + WS_P);
#pragma unroll
  for (int j = 0; j < 4; ++j) {
    if (j < ncnt) {
      f4 v = sel4(kg, acc[j][1], acc[j][0]) + red[(pw * 4 + j) * 64 + lane];
      u32 g0 = pk_bf(v[0], v[1]), g1 = pk_bf(v[2], v[3]);
      g0 = pk_bf(bf_lo(g0) + bias, bf_hi(g0) + bias);
      g1 = pk_bf(bf_lo(g1) + bias, bf_hi(g1) + bias);
      u32x2 o = {pk_bf(bf_lo(g0) * bf_lo(z1v[j][0]), bf_hi(g0) * bf_hi(z1v[j][0])),
                 pk_bf(bf_lo(g1) * bf_lo(z1v[j][1]), bf_hi(g1) * bf_hi(z1v[j][1]))};
      P[((size_t)(t0 + nfirst + j) * 2048 + trow) * 4 + g] = o;
    }
  }
  STAMP(7);
  STAMP_OUT(4);
}

constexpr int G3_NS = 8, G3_A = 10240, G3_STG = 10240 + 8192, G3_LDS = G3_NS * G3_STG;
__global__ __launch_bounds__(512, 2) void k6_gemm3(char* __restrict__ ws, u32x2* __restrict__ out) {
  extern __shared__ __attribute__((aligned(16))) char sm[];
  const int tid = threadIdx.x, lane = tid & 63, w = uni(tid >> 6);
  const int n = lane & 15, g = lane >> 4;
  int rt, cg;
  tile_map(rt, cg);
  const int kg = w >> 2, mh = (w >> 1) & 1, np = (w ^ (w >> 2)) & 1;
  const int nfirst = np ? 3 : 0, ncnt = np ? 2 : 3;
  STAMP_DECL; STAMP(0);
  const int akb = w >> 1;
  const char* asrc = ws + WS_WOUT + ((size_t)cg * 80) * 32 + ((w & 1) * 80 + (lane < 40 ? lane : 0)) * 16;
  const char* bsrc = ws + WS_P + ((size_t)rt * 64) * 32 + ((w & 1) * 64 + lane) * 16;
  const int trow = rt * 64 + (2 * mh + kg) * 16 + n;
  u32x2 hv[3];
#pragma unroll
  for (int j = 0; j < 3; ++j) {
    int jj = j < ncnt ? j : 0;
    hv[j] = ((const u32x2*)(ws + WS_H))[((size_t)(cg * 5 + nfirst + jj) * 2048 + trow) * 4 + g];
  }
  auto issue = [&](int st, int slot) {
    char* base = sm + slot * G3_STG;
    int kb = st * 4 + akb;
    kb = kb < 54 ? kb : 53;
    glds16(bsrc + (size_t)kb * (2048 * 32), base + G3_A + w * 1024);
    if (lane < 40) {
      glds16(asrc + (size_t)kb * (D * 32), base + w * 1280);
      glds16(asrc + (size_t)kb * (D * 32) + 640, base + w * 1280 + 640);
    }
  };
#pragma unroll
  for (int st = 0; st < G3_NS - 1; ++st) issue(st, st);
  const int aoff = (2 * kg + (g >> 1)) * 2560 + (nfirst * 16 + n) * 32 + (g & 1) * 16;
  const int boff = G3_A + (2 * kg + (g >> 1)) * 2048 + (mh * 32 + n) * 32 + (g & 1) * 16;
  f4 acc[3][2];
#pragma unroll
  for (int j = 0; j < 3; ++j) { acc[j][0] = f4{0.f, 0.f, 0.f, 0.f}; acc[j][1] = f4{0.f, 0.f, 0.f, 0.f}; }
  static_for<0, 14>([&](auto ST) {
    constexpr int st = ST.value;
    constexpr int left = (13 - st) < (G3_NS - 2) ? (13 - st) : (G3_NS - 2);
    if constexpr (st == 0) STAMP(1);
    wait_vm<3 * left>(); wait_lgkm0(); raw_barrier();
    if constexpr (st == 0) STAMP(2);
    if constexpr (st == 7) STAMP(3);
    if constexpr (st + G3_NS - 1 < 14) issue(st + G3_NS - 1, (st + G3_NS - 1) % G3_NS);
    if (st < 13 || kg == 0) {
      const char* base = sm + (st % G3_NS) * G3_STG;
      u32x4 b0 = *(const u32x4*)(base + boff), b1 = *(const u32x4*)(base + boff + 512);
#pragma unroll
      for (int j = 0; j < 3; ++j) {
        if (j < ncnt) {
          u32x4 a = *(const u32x4*)(base + aoff + j * 512);
          acc[j][0] = mfma_b(a, b0, acc[j][0]);
          acc[j][1] = mfma_b(a, b1, acc[j][1]);
        }
      }
    }
  });
  STAMP(6);
  wait_lgkm0(); raw_barrier();
  f4* red = (f4*)sm;
#pragma unroll
  for (int j = 0; j < 3; ++j) red[(w * 4 + j) * 64 + lane] = sel4(kg, acc[j][0], acc[j][1]);
  wait_lgkm0(); raw_barrier();
  const int pw = (w ^ 4) ^ 1;
#pragma unroll
  for (int j = 0; j < 3; ++j) {
    if (j < ncnt) {
      f4 v = sel4(kg, acc[j][1], acc[j][0]) + red[(pw * 4 + j) * 64 + lane];
      u32 o0 = pk_bf(v[0], v[1]), o1 = pk_bf(v[2], v[3]);
      u32x2 o = {pk_bf(bf_lo(hv[j][0]) + bf_lo(o0), bf_hi(hv[j][0]) + bf_hi(o0)),
                 pk_bf(bf_lo(hv[j][1]) + bf_lo(o1), bf_hi(hv[j][1]) + bf_hi(o1))};
      out[(size_t)trow * 160 + (cg * 5 + nfirst + j) * 4 + g] = o;
    }
  }
  STAMP(7);
  STAMP_OUT(5);
}

extern "C" void kernel_launch(void* const* d_in, const int* in_sizes, int n_in, void* d_out, int out_size,
                              void* d_ws, size_t ws_size, hipStream_t stream) {
  (void)in_sizes; (void)n_in; (void)out_size; (void)ws_size;
  char* ws = (char*)d_ws;
  (void)hipFuncSetAttribute((const void*)k4_gemm1, hipFuncAttributeMaxDynamicSharedMemorySize, G1_LDS);
  (void)hipFuncSetAttribute((const void*)k5_gemm2, hipFuncAttributeMaxDynamicSharedMemorySize, G2_LDS);
  (void)hipFuncSetAttribute((const void*)k6_gemm3, hipFuncAttributeMaxDynamicSharedMemorySize, G3_LDS);
  k1_fwd<<<256 + 64, 512, K1_LDS, stream>>>((const u32*)d_in[0], (const u32x4*)d_in[1], (const u32x4*)d_in[2], ws);
  k2_col<<<KT + K2_NWIN + K2_NWOUT + K2_NSTAT, 512, K2_LDS, stream>>>((const u32*)d_in[4], (const u32*)d_in[7], ws);
  k3_inv<<<256, 512, K3_LDS, stream>>>((const u32x2*)d_in[0], ws);
  k4_gemm1<<<256, 512, G1_LDS, stream>>>(ws);
  k5_gemm2<<<256, 512, G2_LDS, stream>>>((const char*)d_in[5], (const u16*)d_in[6], ws);
  k6_gemm3<<<256, 512, G3_LDS, stream>>>(ws, (u32x2*)d_out);
}
```

```cpp
#include <hip/hip_runtime.h>
#include <stdint.h>

typedef unsigned int u32;
typedef unsigned short u16;
typedef unsigned long long u64;
typedef _Float16 h2 __attribute__((ext_vector_type(2)));
typedef _Float16 h4 __attribute__((ext_vector_type(4)));
typedef _Float16 h8 __attribute__((ext_vector_type(8)));
typedef short s4 __attribute__((ext_vector_type(4)));
typedef __bf16 b2 __attribute__((ext_vector_type(2)));
typedef __bf16 b8 __attribute__((ext_vector_type(8)));
typedef float f2 __attribute__((ext_vector_type(2)));
typedef float f4 __attribute__((ext_vector_type(4)));
typedef u32 u32x2 __attribute__((ext_vector_type(2)));
typedef u32 u32x4 __attribute__((ext_vector_type(4)));
#define DI __device__ __forceinline__
#define LDS_AS __attribute__((address_space(3)))
#define GLB_AS __attribute__((address_space(1)))

constexpr int S = 2048, D = 640, FH = 864, F = 1728, KT = 321;

constexpr size_t MB = 1u << 20;
constexpr size_t WS_T1 = 0 * MB;
constexpr size_t WS_T2 = 3 * MB;
constexpr size_t WS_WT = 6 * MB;
constexpr size_t WS_H = 9 * MB;
constexpr size_t WS_HN = 12 * MB;
constexpr size_t WS_WIN = 15 * MB;
constexpr size_t WS_WOUT = 18 * MB;
constexpr size_t WS_Z1 = 20 * MB;
constexpr size_t WS_Z2 = 24 * MB;
constexpr size_t WS_P = 28 * MB;
constexpr size_t WS_STAT = 32 * MB;
constexpr size_t WS_WMAX = 33 * MB;

constexpr double kPi = 3.14159265358979323846264338327950288;
constexpr double c_sin_small(double x) {
  double t = x, s = x;
  for (int i = 1; i < 10; ++i) { t *= -x * x / ((2 * i) * (2 * i + 1)); s += t; }
  return s;
}
constexpr double c_cos_small(double x) {
  double t = 1, s = 1;
  for (int i = 1; i < 10; ++i) { t *= -x * x / ((2 * i - 1) * (2 * i)); s += t; }
  return s;
}
struct Cos640 { double c[640]; };
constexpr Cos640 make_cos640() {
  Cos640 t{};
  for (int j = 0; j <= 160; ++j) {
    double v = (j <= 80) ? c_cos_small(2 * kPi * j / 640) : c_sin_small(2 * kPi * (160 - j) / 640);
    if (j == 160) v = 0.0;
    t.c[j] = v;
    t.c[(640 - j) % 640] = v;
    t.c[320 - j] = -v;
    t.c[(320 + j) % 640] = -v;
  }
  return t;
}
constexpr Cos640 kCos = make_cos640();
constexpr double cs(int j) { return kCos.c[((j % 640) + 640) % 640]; }
constexpr double sn(int j) { return kCos.c[(((j - 160) % 640) + 640) % 640]; }
constexpr u32 pkh(double lo, double hi) {
  return (u32)__builtin_bit_cast(u16, (_Float16)(float)lo) | ((u32)__builtin_bit_cast(u16, (_Float16)(float)hi) << 16);
}
constexpr u32 fb(double v) { return __builtin_bit_cast(u32, (float)v); }
constexpr double kRs40 = 0.15811388300841896659994467722163593;

struct Tab { u32 v[12][64][4]; };
constexpr Tab make_tab_fwd() {
  Tab t{};
  for (int l = 0; l < 64; ++l) {
    int n = l & 15, g = l >> 4;
    for (int nt = 0; nt < 2; ++nt) {
      int k1 = 16 * nt + n;
      bool ok = k1 <= 20;
      for (int q = 0; q < 4; ++q) {
        int a0 = (q < 2) ? 4 * g + 2 * q : 16 + 4 * g + 2 * (q - 2), a1 = a0 + 1;
        t.v[0 + nt][l][q] = ok ? pkh(cs(16 * a0 * k1), cs(16 * a1 * k1)) : 0u;
        t.v[2 + nt][l][q] = ok ? pkh(-sn(16 * a0 * k1), -sn(16 * a1 * k1)) : 0u;
      }
      for (int q = 0; q < 2; ++q) {
        int a0 = 32 + 4 * g + 2 * q, a1 = a0 + 1;
        bool v = ok && a0 < 40;
        t.v[4][l][2 * nt + q] = v ? pkh(cs(16 * a0 * k1), cs(16 * a1 * k1)) : 0u;
        t.v[5][l][2 * nt + q] = v ? pkh(-sn(16 * a0 * k1), -sn(16 * a1 * k1)) : 0u;
      }
      for (int r = 0; r < 4; ++r) {
        int b = 4 * g + r;
        t.v[6 + 2 * nt][l][r] = fb(cs(b * k1) * kRs40);
        t.v[7 + 2 * nt][l][r] = fb(-sn(b * k1) * kRs40);
      }
    }
    for (int q = 0; q < 4; ++q) {
      int b = 4 * g + q, k2 = n;
      double c = cs(40 * b * k2) * 0.25, s = sn(40 * b * k2) * 0.25, sg = (k2 >= 8) ? -1.0 : 1.0;
      t.v[10][l][q] = pkh(c, s);
      t.v[11][l][q] = pkh(-s * sg, c * sg);
    }
  }
  return t;
}
constexpr Tab make_tab_inv() {
  Tab t{};
  for (int l = 0; l < 64; ++l) {
    int n = l & 15, g = l >> 4;
    for (int q = 0; q < 4; ++q) {
      int k2 = 4 * g + q, b = n;
      double c = cs(40 * b * k2) * 0.25, s = sn(40 * b * k2) * 0.25, sg = (k2 >= 8) ? -1.0 : 1.0;
      t.v[0][l][q] = pkh(c, -s * sg);
      t.v[1][l][q] = pkh(s, c * sg);
    }
    for (int mt = 0; mt < 2; ++mt) {
      for (int r = 0; r < 4; ++r) {
        int k1 = 16 * mt + 4 * g + r, b = n;
        t.v[2 + 2 * mt][l][r] = fb(cs(b * k1) * kRs40);
        t.v[3 + 2 * mt][l][r] = fb(sn(b * k1) * kRs40);
      }
      for (int nt = 0; nt < 3; ++nt) {
        int a = 16 * nt + n;
        for (int q = 0; q < 4; ++q) {
          int k1 = 16 * mt + 4 * g + q;
          double w = (k1 == 0 || k1 == 20) ? 1.0 : 2.0;
          bool ok = k1 <= 20 && a < 40;
          t.v[6 + 2 * nt + mt][l][q] = ok ? pkh(w * cs(16 * a * k1), -w * sn(16 * a * k1)) : 0u;
        }
      }
    }
  }
  return t;
}
static __device__ constexpr Tab g_tab_fwd = make_tab_fwd();
static __device__ constexpr Tab g_tab_inv = make_tab_inv();

#define STAMP_DECL do {} while (0)
#define STAMP(I) do {} while (0)
#define STAMP_OUT(K) do {} while (0)
DI float bf_lo(u32 v) { return __builtin_bit_cast(float, v << 16); }
DI float bf_hi(u32 v) { return __builtin_bit_cast(float, v & 0xffff0000u); }
DI u32 pk_bf(float a, float b) { f2 x = {a, b}; return __builtin_bit_cast(u32, __builtin_convertvector(x, b2)); }
DI u32 pk_h(float a, float b) { f2 x = {a, b}; return __builtin_bit_cast(u32, __builtin_convertvector(x, h2)); }
DI float rbf(float a) { return bf_lo(pk_bf(a, a)); }
template <int CTRL> DI float dpp(float v) {
  return __builtin_bit_cast(float, __builtin_amdgcn_update_dpp(0, __builtin_bit_cast(int, v), CTRL, 0xf, 0xf, false));
}
DI float row16_sum(float v) {
  v += dpp<0x128>(v); v += dpp<0x124>(v); v += dpp<0x122>(v); v += dpp<0x121>(v);
  return v;
}
DI float wave_sum(float v) {
  v = row16_sum(v);
  int x = __builtin_bit_cast(int, v);
  float a = __builtin_bit_cast(float, __builtin_amdgcn_readlane(x, 0));
  float b = __builtin_bit_cast(float, __builtin_amdgcn_readlane(x, 16));
  float c = __builtin_bit_cast(float, __builtin_amdgcn_readlane(x, 32));
  float d = __builtin_bit_cast(float, __builtin_amdgcn_readlane(x, 48));
  return (a + b) + (c + d);
}
DI u32x4 cmul_pk(f4 re, f4 im, f4 tx, f4 ty) {
  f4 a = re * tx - im * ty, b = re * ty + im * tx;
  return u32x4{pk_h(a[0], b[0]), pk_h(a[1], b[1]), pk_h(a[2], b[2]), pk_h(a[3], b[3])};
}
DI float opq(float v) { asm("" : "+v"(v)); return v; }
DI float h_lo(u32 v) { return (float)__builtin_bit_cast(h2, v)[0]; }
DI float h_hi(u32 v) { return (float)__builtin_bit_cast(h2, v)[1]; }
template <int SG> DI void radix8_half(int e, f4 ar, f4 ai, f4 br, f4 bi, f4& xr, f4& xi) {
  f4 pr, pi;
  if (e == 0) {
    pr = ar + br; pi = ai + bi;
  } else {
    const float r2 = 0.70710678118654752f;
    const f4 cx = {1.f, r2, 0.f, -r2}, cy = {0.f, SG * r2, SG * 1.f, SG * r2};
    f4 dr = ar - br, di = ai - bi;
    pr = dr * cx - di * cy; pi = dr * cy + di * cx;
  }
  f2 sr = pr.xy + pr.zw, si = pi.xy + pi.zw;
  f2 dr = pr.xy - pr.zw, di = pi.xy - pi.zw;
  xr[0] = opq(sr.x + sr.y); xi[0] = opq(si.x + si.y);
  xr[2] = opq(sr.x - sr.y); xi[2] = opq(si.x - si.y);
  if (SG < 0) {
    xr[1] = opq(dr.x + di.y); xi[1] = opq(di.x - dr.y);
    xr[3] = opq(dr.x - di.y); xi[3] = opq(di.x + dr.y);
  } else {
    xr[1] = opq(dr.x - di.y); xi[1] = opq(di.x + dr.y);
    xr[3] = opq(dr.x + di.y); xi[3] = opq(di.x - dr.y);
  }
}
DI f4 mfma_h(u32x4 a, u32x4 b, f4 c) {
  return __builtin_amdgcn_mfma_f32_16x16x32_f16(__builtin_bit_cast(h8, a), __builtin_bit_cast(h8, b), c, 0, 0, 0);
}
DI f4 mfma_b(u32x4 a, u32x4 b, f4 c) {
  return __builtin_amdgcn_mfma_f32_16x16x32_bf16(__builtin_bit_cast(b8, a), __builtin_bit_cast(b8, b), c, 0, 0, 0);
}
DI u32x2 tr_read(const char* p) {
  return __builtin_bit_cast(u32x2, __builtin_amdgcn_ds_read_tr16_b64_v4i16((LDS_AS s4*)p));
}
DI void glds16(const void* g, char* lds_wave_base) {
  __builtin_amdgcn_global_load_lds((const GLB_AS u32*)g, (LDS_AS u32*)lds_wave_base, 16, 0, 0);
}
DI void pf_lds(const void* g, u32 lds) {
  u32 keep;
  asm volatile("s_mov_b32 %0, m0\n\ts_mov_b32 m0, %2\n\ts_nop 0\n\tglobal_load_lds_dword %1, off\n\ts_mov_b32 m0, %0"
               : "=&s"(keep) : "v"(g), "s"(lds) : "memory");
}
template <int N> DI void wait_vm() { asm volatile("s_waitcnt vmcnt(%0)" ::"n"(N) : "memory"); }
DI void wait_lgkm0() { asm volatile("s_waitcnt lgkmcnt(0)" ::: "memory"); }
DI void raw_barrier() { __builtin_amdgcn_s_barrier(); }
DI f4 sel4(bool c, f4 a, f4 b) { return f4{c ? a[0] : b[0], c ? a[1] : b[1], c ? a[2] : b[2], c ? a[3] : b[3]}; }
DI int uni(int v) { return __builtin_amdgcn_readfirstlane(v); }

DI f2 ln_scalars(float mean, float var) {
  float m = rbf(mean);
  float v = rbf(var);
  float den = rbf(__builtin_sqrtf(rbf(v + 1e-5f)));
  return f2{m, 1.0f / den};
}
DI float gelu_f(float u) {
  const float c1 = -2.0f * 0.7978845608028654f * 1.4426950408889634f;
  const float c2 = c1 * 0.044715f;
  float e = __builtin_amdgcn_exp2f(u * (c1 + c2 * u * u));
  return u * __builtin_amdgcn_rcpf(1.0f + e);
}
DI f2 w_scales(const u32* wmax, int lane) {
  u32 m = wmax[lane];
  for (int o = 32; o > 0; o >>= 1) { u32 t = __shfl_xor(m, o); m = m > t ? m : t; }
  int e = (int)(m >> 7);
  e = e < 4 ? 4 : (e > 254 ? 254 : e);
  return f2{__builtin_bit_cast(float, (u32)(256 - e) << 23), __builtin_bit_cast(float, (u32)(e - 2) << 23)};
}

constexpr int K1_TAB = 0, K1_XN = 12288, K1_STG = 24576, K1_LDS = 41216;
__global__ __launch_bounds__(512) void k1_fwd(const u32* __restrict__ x, const u32x4* __restrict__ wr,
                                              const u32x4* __restrict__ wi, char* __restrict__ ws) {
  extern __shared__ __attribute__((aligned(16))) char sm[];
  const int tid = threadIdx.x, lane = tid & 63, w = uni(tid >> 6);
  const int n = lane & 15, g = lane >> 4;
  {
    u64 pc;
    asm volatile("s_getpc_b64 %0" : "=s"(pc));
    const u32 scr = (u32)(size_t)(LDS_AS char*)sm + K1_LDS;
    if (w == 7 && blockIdx.x < 256) {
      const char* cp = (const char*)(pc & ~255ull) + (blockIdx.x >> 3) * 1056 + (lane < 9 ? lane : 8) * 128;
      pf_lds(cp, scr);
    }
    if (w == 6 && blockIdx.x < 256) {
      const char* cp = (const char*)&g_tab_inv + (blockIdx.x >> 3) * 384 + (lane < 3 ? lane : 2) * 128;
      pf_lds(cp, scr);
    }
  }
  STAMP_DECL; STAMP(0);
  if (blockIdx.x >= 256) {
    const int q = blockIdx.x - 256;
    const int jbase = 8 * (q & 15) + 512 * (q >> 4);
    u32 mx = 0;
    for (int c = tid; c < 8 * 321; c += 512) {
      int part = c / 321, off = c - part * 321;
      const u32x4* src = (part >> 2) ? wi : wr;
      u32x4 v = src[(size_t)((jbase + 128 * (part & 3)) >> 3) * 321 + off];
      *(u32x4*)(sm + c * 16) = v;
#pragma unroll
      for (int i = 0; i < 4; ++i) {
        u32 a = v[i] & 0x7fffu, b = (v[i] >> 16) & 0x7fffu;
        mx = mx > a ? mx : a;
        mx = mx > b ? mx : b;
      }
    }
    for (int o = 32; o > 0; o >>= 1) { u32 t = __shfl_xor(mx, o); mx = mx > t ? mx : t; }
    u32* red = (u32*)(sm + 41088);
    if (lane == 0) red[w] = mx;
    STAMP(1);
    __syncthreads();
    STAMP(2);
    if (tid == 0) {
      u32 m = 0;
      for (int i = 0; i < 8; ++i) m = m > red[i] ? m : red[i];
      ((u32*)(ws + WS_WMAX))[q] = m;
    }
    const u16* s16 = (const u16*)sm;
    u32x4* dst = (u32x4*)(ws + WS_WT);
    for (int c = tid; c < 8 * 321; c += 512) {
      int k = c >> 3, ww = c & 7;
      u32x4 o;
#pragma unroll
      for (int r = 0; r < 4; ++r)
        o[r] = (u32)s16[r * 2568 + ww * 321 + k] | ((u32)s16[(4 + r) * 2568 + ww * 321 + k] << 16);
      dst[(size_t)k * 512 + q * 8 + ww] = o;
    }
    STAMP_OUT(0);
    return;
  }
  const int b = blockIdx.x;
  const int rg = (b & 7) * 32 + (b >> 3);
  const int s = rg * 8 + w;
  u32 xv[5];
#pragma unroll
  for (int i = 0; i < 5; ++i) xv[i] = x[(size_t)s * 320 + i * 64 + lane];
  glds16(&g_tab_fwd.v[w][lane][0], sm + K1_TAB + w * 1024);
  if (w < 4) glds16(&g_tab_fwd.v[8 + w][lane][0], sm + K1_TAB + (8 + w) * 1024);
  STAMP(1);
  wait_vm<0>();
  STAMP(2);
  f2 v[5];
#pragma unroll
  for (int i = 0; i < 5; ++i) v[i] = f2{bf_lo(xv[i]), bf_hi(xv[i])};
  const f2 s2 = ((v[0] + v[1]) + (v[2] + v[3])) + v[4];
  const float mean = wave_sum(opq(s2.x) + opq(s2.y)) * (1.0f / 640.0f);
  f2 q2 = {0.f, 0.f};
#pragma unroll
  for (int i = 0; i < 5; ++i) { f2 d = v[i] - mean; q2 += d * d; }
  const float var = wave_sum(opq(q2.x) + opq(q2.y)) * (1.0f / 640.0f);
  const f2 ln = ln_scalars(mean, var);
  char* xn = sm + K1_XN + w * 1536;
#pragma unroll
  for (int i = 0; i < 5; ++i) {
    u32 d = pk_bf(v[i].x - ln.x, v[i].y - ln.x);
    u32 o = pk_bf(bf_lo(d) * ln.y, bf_hi(d) * ln.y);
    *(u32*)(xn + (i * 64 + lane) * 4) = pk_h(bf_lo(o), bf_hi(o));
  }
  *(u32*)(xn + (320 + lane) * 4) = 0u;
  STAMP(3);
  __syncthreads();
  STAMP(4);
  const u32x4* tab = (const u32x4*)(sm + K1_TAB) + lane;
  u32x2 a0 = tr_read(xn + lane * 8), a1 = tr_read(xn + 512 + lane * 8), a2 = tr_read(xn + 1024 + lane * 8);
  u32x4 af = {a0[0], a0[1], a1[0], a1[1]};
  u32x4 tre = tab[4 * 64], tim = tab[5 * 64], fr = tab[10 * 64], fi = tab[11 * 64];
  u32* stg = (u32*)(sm + K1_STG) + w * 328;
#pragma unroll
  for (int nt = 0; nt < 2; ++nt) {
    f4 z = {0.f, 0.f, 0.f, 0.f};
    f4 re = mfma_h(af, tab[(0 + nt) * 64], z);
    f4 im = mfma_h(af, tab[(2 + nt) * 64], z);
    const u32x4 at = nt ? u32x4{0u, 0u, a2[0], a2[1]} : u32x4{a2[0], a2[1], 0u, 0u};
    re = mfma_h(at, tre, re);
    im = mfma_h(at, tim, im);
    u32x4 fb_ = cmul_pk(re, im, __builtin_bit_cast(f4, tab[(6 + 2 * nt) * 64]), __builtin_bit_cast(f4, tab[(7 + 2 * nt) * 64]));
    f4 ore = mfma_h(fr, fb_, z), oim = mfma_h(fi, fb_, z);
    const int k1 = 16 * nt + n;
#pragma unroll
    for (int r = 0; r < 4; ++r) {
      int k2 = 4 * g + r, k = k1 + 40 * k2;
      int kt = k2 < 8 ? k : 640 - k;
      bool ok = k1 <= 20 && !(k1 == 0 && k2 > 8) && !(k1 == 20 && k2 >= 8);
      if (ok) stg[kt] = pk_h(ore[r], oim[r]);
    }
  }
  STAMP(5);
  __syncthreads();
  STAMP(6);
  if (tid < KT) {
    const u32* sp = (const u32*)(sm + K1_STG) + tid;
    u32x4 o0 = {sp[0], sp[328], sp[2 * 328], sp[3 * 328]};
    u32x4 o1 = {sp[4 * 328], sp[5 * 328], sp[6 * 328], sp[7 * 328]};
    u32x4* dst = (u32x4*)(ws + WS_T1) + (size_t)tid * 512 + rg * 2;
    dst[0] = o0;
    dst[1] = o1;
  }
  STAMP(7);
  STAMP_OUT(0);
}

constexpr int K2_LDS = 10240 + 16384;
constexpr int K2_NWIN = 68, K2_NWOUT = 34, K2_NSTAT = 8;
DI void transpose16(const u32* __restrict__ src, u32x4* __restrict__ dst, int item, int npair, int nitems) {
  if (item >= nitems) return;
  int kb = item / npair, p = item - kb * npair;
  u32 v[16];
#pragma unroll
  for (int i = 0; i < 16; ++i) v[i] = src[(size_t)(kb * 16 + i) * npair + p];
  u32x4 o[4];
#pragma unroll
  for (int i = 0; i < 8; ++i) {
    o[i >> 2][i & 3] = __builtin_amdgcn_perm(v[2 * i + 1], v[2 * i], 0x05040100u);
    o[2 + (i >> 2)][i & 3] = __builtin_amdgcn_perm(v[2 * i + 1], v[2 * i], 0x07060302u);
  }
  u32x4* d = dst + (size_t)item * 4;
  d[0] = o[0]; d[1] = o[1]; d[2] = o[2]; d[3] = o[3];
}
__global__ __launch_bounds__(512) void k2_col(const u32* __restrict__ w_in, const u32* __restrict__ w_out,
                                              char* __restrict__ ws, const char* __restrict__ wsp) {
  extern __shared__ __attribute__((aligned(16))) char sm[];
  const int tid = threadIdx.x, lane = tid & 63, w = uni(tid >> 6);
  const int n = lane & 15, g = lane >> 4;
  STAMP_DECL; STAMP(0);
  if (blockIdx.x >= KT) {
    int q = blockIdx.x - KT;
    if (q < K2_NWIN) {
      transpose16(w_in, (u32x4*)(ws + WS_WIN), q * 512 + tid, F / 2, 40 * (F / 2));
    } else if (q < K2_NWIN + K2_NWOUT) {
      transpose16(w_out, (u32x4*)(ws + WS_WOUT), (q - K2_NWIN) * 512 + tid, D / 2, 54 * (D / 2));
    } else if (q >= K2_NWIN + K2_NWOUT + K2_NSTAT) {
      const char* cp = wsp + (size_t)((q - K2_NWIN - K2_NWOUT - K2_NSTAT) * 512 + tid) * 128;
      pf_lds(cp, (u32)(size_t)(LDS_AS char*)sm);
      wait_vm<0>();
    } else {
      u32x4* st = (u32x4*)(ws + WS_STAT) + (q - K2_NWIN - K2_NWOUT) * 2048 + tid;
      u32x4 f = {~0u, ~0u, ~0u, ~0u};
      st[0] = f; st[512] = f; st[1024] = f; st[1536] = f;
    }
    STAMP_OUT(1);
    return;
  }
  const int kt = blockIdx.x;
  const int bp = tid & 255, e = w >> 2;
  const u32* t1 = (const u32*)(ws + WS_T1) + (size_t)kt * 2048 + bp;
  u32 uv[8];
#pragma unroll
  for (int a = 0; a < 8; ++a) uv[a] = t1[a * 256];
  const u32x4 wv = *((const u32x4*)(ws + WS_WT) + (size_t)kt * 512 + lane * 8 + w);
  const f2 wsc = w_scales((const u32*)(ws + WS_WMAX), lane);
  const float rs8 = 0.35355339059327376f;
  f4 tax, tay, t1x, t1y, t3x, t3y;
  u32x4 Fr, Fi, Gr, Gi;
#pragma unroll
  for (int i = 0; i < 4; ++i) {
    float a0 = (float)(bp * (2 * i + e)) * (1.0f / 2048.0f);
    float a1 = (float)((4 * g + i) * n) * (1.0f / 256.0f);
    float a3 = (float)((16 * (4 * g + i) + n) * w) * (1.0f / 2048.0f);
    float a4 = (float)(((4 * g + i) * n) & 15) * (1.0f / 16.0f);
    tax[i] = __builtin_amdgcn_cosf(a0); tay[i] = __builtin_amdgcn_sinf(a0);
    t1x[i] = __builtin_amdgcn_cosf(a1); t1y[i] = __builtin_amdgcn_sinf(a1);
    t3x[i] = __builtin_amdgcn_cosf(a3); t3y[i] = __builtin_amdgcn_sinf(a3);
    float c = __builtin_amdgcn_cosf(a4), sn_ = __builtin_amdgcn_sinf(a4);
    Fr[i] = pk_h(c, sn_); Fi[i] = pk_h(-sn_, c);
    Gr[i] = pk_h(c, -sn_); Gi[i] = pk_h(sn_, c);
  }
  STAMP(1);
  wait_vm<0>();
  STAMP(2);
  tax *= rs8; tay *= -rs8; t1x *= 0.25f; t1y *= 0.25f; t3x *= 0.25f * rs8; t3y *= 0.25f * rs8;
  {
    f4 ar, ai, br, bi, xr, xi;
#pragma unroll
    for (int a = 0; a < 4; ++a) { ar[a] = h_lo(uv[a]); ai[a] = h_hi(uv[a]); br[a] = h_lo(uv[a + 4]); bi[a] = h_hi(uv[a + 4]); }
    radix8_half<-1>(e, ar, ai, br, bi, xr, xi);
    u32x4 o = cmul_pk(xr, xi, tax, tay);
    u32* ex = (u32*)sm + (bp & 15) * 20 + (bp >> 4) + e * 320;
#pragma unroll
    for (int m = 0; m < 4; ++m) ex[m * 640] = o[m];
  }
  STAMP(3);
  __syncthreads();
  STAMP(4);
  const f4 z = {0.f, 0.f, 0.f, 0.f};
  u32x4 fa = *(const u32x4*)(sm + (w * 320 + n * 20 + 4 * g) * 4);
  f4 re = mfma_h(fa, Fr, z), im = mfma_h(fa, Fi, z);
  fa = cmul_pk(re, im, t1x, -t1y);
  re = mfma_h(Fr, fa, z); im = mfma_h(Fi, fa, z);
  {
    const float wk = 0.25f * wsc.x;
    f4 wx, wy;
#pragma unroll
    for (int r = 0; r < 4; ++r) { wx[r] = bf_lo(wv[r]); wy[r] = bf_hi(wv[r]); }
    fa = cmul_pk(re, im, wx * wk, wy * wk);
  }
  re = mfma_h(fa, Gr, z); im = mfma_h(fa, Gi, z);
  fa = cmul_pk(re, im, t1x, t1y);
  re = mfma_h(Gr, fa, z); im = mfma_h(Gi, fa, z);
  {
    float* ex = (float*)(sm + 10240) + w * 256 + 64 * g + n;
    f4 a = re * t3x - im * t3y, b = re * t3y + im * t3x;
#pragma unroll
    for (int r = 0; r < 4; ++r) { ex[16 * r] = a[r]; ex[2048 + 16 * r] = b[r]; }
  }
  STAMP(5);
  __syncthreads();
  STAMP(6);
  {
    const float* ex = (const float*)(sm + 10240) + bp;
    f4 ar, ai, br, bi, xr, xi;
#pragma unroll
    for (int j = 0; j < 4; ++j) {
      ar[j] = ex[j * 256]; br[j] = ex[(j + 4) * 256];
      ai[j] = ex[2048 + j * 256]; bi[j] = ex[2048 + (j + 4) * 256];
    }
    radix8_half<1>(e, ar, ai, br, bi, xr, xi);
    u32* t2p = (u32*)(ws + WS_T2) + (size_t)kt * 2048 + bp + e * 256;
#pragma unroll
    for (int m = 0; m < 4; ++m) t2p[m * 512] = pk_h(xr[m], xi[m]);
  }
  STAMP(7);
  STAMP_OUT(1);
}

constexpr int K3_TAB = 0, K3_Z = 12288, K3_ZSTR = 340, K3_LDS = 12288 + 8 * K3_ZSTR * 4;
__global__ __launch_bounds__(512) void k3_inv(const u32x2* __restrict__ x, char* __restrict__ ws) {
  extern __shared__ __attribute__((aligned(16))) char sm[];
  const int tid = threadIdx.x, lane = tid & 63, w = uni(tid >> 6);
  const int n = lane & 15, g = lane >> 4;
  const int b = blockIdx.x;
  const int rg = (b & 7) * 32 + (b >> 3);
  const int s = rg * 8 + w;
  STAMP_DECL; STAMP(0);
  u32x4 z0, z1;
  if (tid < 336) {
    int k1 = tid >> 4, k2 = tid & 15, k = k1 + 40 * k2;
    int kt = k2 < 8 ? k : 640 - k;
    const u32x4* src = (const u32x4*)(ws + WS_T2) + (size_t)kt * 512 + rg * 2;
    z0 = src[0]; z1 = src[1];
  }
  u32x2 xv[3];
#pragma unroll
  for (int nt = 0; nt < 3; ++nt) {
    int a = 16 * nt + n;
    a = a < 40 ? a : 39;
    xv[nt] = x[(size_t)s * 160 + a * 4 + g];
  }
  const f2 wsc = w_scales((const u32*)(ws + WS_WMAX), lane);
  glds16(&g_tab_inv.v[w][lane][0], sm + K3_TAB + w * 1024);
  if (w < 4) glds16(&g_tab_inv.v[8 + w][lane][0], sm + K3_TAB + (8 + w) * 1024);
  STAMP(1);
  wait_vm<0>();
  STAMP(2);
  if (tid < 336) {
    u32* zp = (u32*)(sm + K3_Z) + tid;
#pragma unroll
    for (int i = 0; i < 4; ++i) { zp[i * K3_ZSTR] = z0[i]; zp[(4 + i) * K3_ZSTR] = z1[i]; }
  }
  STAMP(3);
  __syncthreads();
  STAMP(4);
  const u32x4* tab = (const u32x4*)(sm + K3_TAB) + lane;
  const f4 z = {0.f, 0.f, 0.f, 0.f};
  u32x4 gr = tab[0], gi = tab[64];
  u32x4 fv[2];
#pragma unroll
  for (int mt = 0; mt < 2; ++mt) {
    int k1 = 16 * mt + n;
    k1 = k1 <= 20 ? k1 : 20;
    u32x4 fa = *(const u32x4*)(sm + K3_Z + (w * K3_ZSTR + k1 * 16 + 4 * g) * 4);
    f4 re = mfma_h(fa, gr, z), im = mfma_h(fa, gi, z);
    fv[mt] = cmul_pk(re, im, __builtin_bit_cast(f4, tab[(2 + 2 * mt) * 64]), __builtin_bit_cast(f4, tab[(3 + 2 * mt) * 64]));
  }
  float hv[12];
  u32x2 hp[3];
  float sum = 0.f;
  const bool v2 = n < 8;
#pragma unroll
  for (int nt = 0; nt < 3; ++nt) {
    f4 y = mfma_h(fv[0], tab[(6 + 2 * nt) * 64], z);
    y = mfma_h(fv[1], tab[(7 + 2 * nt) * 64], y);
    u32 y0 = pk_bf(y[0] * wsc.y, y[1] * wsc.y), y1 = pk_bf(y[2] * wsc.y, y[3] * wsc.y);
    hp[nt][0] = pk_bf(bf_lo(xv[nt][0]) + bf_lo(y0), bf_hi(xv[nt][0]) + bf_hi(y0));
    hp[nt][1] = pk_bf(bf_lo(xv[nt][1]) + bf_lo(y1), bf_hi(xv[nt][1]) + bf_hi(y1));
    hv[4 * nt + 0] = bf_lo(hp[nt][0]); hv[4 * nt + 1] = bf_hi(hp[nt][0]);
    hv[4 * nt + 2] = bf_lo(hp[nt][1]); hv[4 * nt + 3] = bf_hi(hp[nt][1]);
    float t = (hv[4 * nt] + hv[4 * nt + 1]) + (hv[4 * nt + 2] + hv[4 * nt + 3]);
    sum += (nt < 2 || v2) ? t : 0.f;
  }
  const float mean = wave_sum(sum) * (1.0f / 640.0f);
  float sq = 0.f;
#pragma unroll
  for (int nt = 0; nt < 3; ++nt) {
    float t = 0.f;
#pragma unroll
    for (int r = 0; r < 4; ++r) { float d = hv[4 * nt + r] - mean; t += d * d; }
    sq += (nt < 2 || v2) ? t : 0.f;
  }
  const float var = wave_sum(sq) * (1.0f / 640.0f);
  const f2 ln = ln_scalars(mean, var);
  STAMP(5);
  u32x2* hb = (u32x2*)(ws + WS_H);
  u32x2* hn = (u32x2*)(ws + WS_HN);
#pragma unroll
  for (int nt = 0; nt < 3; ++nt) {
    if (nt < 2 || v2) {
      u32 d0 = pk_bf(hv[4 * nt] - ln.x, hv[4 * nt + 1] - ln.x), d1 = pk_bf(hv[4 * nt + 2] - ln.x, hv[4 * nt + 3] - ln.x);
      u32x2 o = {pk_bf(bf_lo(d0) * ln.y, bf_hi(d0) * ln.y), pk_bf(bf_lo(d1) * ln.y, bf_hi(d1) * ln.y)};
      size_t idx = ((size_t)(16 * nt + n) * 2048 + s) * 4 + g;
      hb[idx] = hp[nt];
      hn[idx] = o;
    }
  }
  STAMP(7);
  STAMP_OUT(2);
}

DI void tile_map(int& rt, int& cg) {
  int b = blockIdx.x, x = b & 7, i = b >> 3;
  rt = (x >> 1) * 8 + (i >> 2);
  cg = (x & 1) * 4 + (i & 3);
}
DI int cg_start(int cg) { return cg * 7 - (cg >> 2); }
DI int cg_count(int cg) { return (cg & 3) == 3 ? 6 : 7; }

template <int I, int N, class Fn> DI void static_for(Fn&& f) {
  if constexpr (I < N) { f(std::integral_constant<int, I>{}); static_for<I + 1, N>(f); }
}

constexpr int G1_NS = 8, G1_STG = 14336, G1_RED = G1_NS * G1_STG, G1_LDS = G1_RED + 32768;
__global__ __launch_bounds__(512, 2) void k4_gemm1(char* __restrict__ ws) {
  extern __shared__ __attribute__((aligned(16))) char sm[];
  const int tid = threadIdx.x, lane = tid & 63, w = uni(tid >> 6);
  const int n = lane & 15, g = lane >> 4;
  int rt, cg;
  tile_map(rt, cg);
  const int nt_all = cg_count(cg), t0 = cg_start(cg);
  const int kg = w >> 2, mh = (w >> 1) & 1, np = (w ^ (w >> 2)) & 1;
  const int nfirst = np ? nt_all - 3 : 0, ncnt = np ? 3 : nt_all - 3;
  const int rows = nt_all * 16, per = rows >> 1;
  STAMP_DECL; STAMP(0);
  const char* bsrc[2];
#pragma unroll
  for (int i = 0; i < 2; ++i) {
    int c = (2 * w + i) * per + (lane < per ? lane : 0);
    int kb = c / (2 * rows), rem = c - kb * 2 * rows;
    bsrc[i] = ws + WS_WIN + ((size_t)kb * F + t0 * 16) * 32 + rem * 16;
  }
  auto issue = [&](int st, int slot) {
    size_t off = st < 10 ? (size_t)st * (4 * F * 32) + FH * 32 : (size_t)(st - 10) * (4 * F * 32);
    if (lane < per) {
      glds16(bsrc[0] + off, sm + slot * G1_STG + (2 * w) * per * 16);
      glds16(bsrc[1] + off, sm + slot * G1_STG + (2 * w + 1) * per * 16);
    }
  };
  u32x4 af[10][2];
  {
    const char* ap = ws + WS_HN + ((size_t)(2 * kg + (g >> 1)) * 2048 + rt * 64 + mh * 32 + n) * 32 + (g & 1) * 16;
#pragma unroll
    for (int st = 0; st < 10; ++st)
#pragma unroll
      for (int m = 0; m < 2; ++m) af[st][m] = *(const u32x4*)(ap + (size_t)st * (4 * 2048 * 32) + m * 512);
  }
#pragma unroll
  for (int st = 0; st < G1_NS - 1; ++st) issue(st, st);
  const int boff = (2 * kg + (g >> 1)) * rows * 32 + (nfirst * 16 + n) * 32 + (g & 1) * 16;
  f4 acc[4][2];
  u32 z2p[4][2];
  const int srow = rt * 64 + (2 * mh + kg) * 16;
  auto reduce = [&]() {
    f4* red = (f4*)(sm + G1_RED);
#pragma unroll
    for (int j = 0; j < 4; ++j) red[(w * 4 + j) * 64 + lane] = sel4(kg, acc[j][0], acc[j][1]);
    wait_lgkm0(); raw_barrier();
    const int pw = (w ^ 4) ^ 1;
#pragma unroll
    for (int j = 0; j < 4; ++j) acc[j][0] = sel4(kg, acc[j][1], acc[j][0]) + red[(pw * 4 + j) * 64 + lane];
  };
  static_for<0, 20>([&](auto ST) {
    constexpr int st = ST.value;
    constexpr int left = (19 - st) < (G1_NS - 2) ? (19 - st) : (G1_NS - 2);
    if constexpr (st == 0 || st == 10) {
#pragma unroll
      for (int j = 0; j < 4; ++j) { acc[j][0] = f4{0.f, 0.f, 0.f, 0.f}; acc[j][1] = f4{0.f, 0.f, 0.f, 0.f}; }
    }
    if constexpr (st == 0) STAMP(1);
    wait_vm<2 * left>(); wait_lgkm0(); raw_barrier();
    if constexpr (st == 0) STAMP(2);
    if constexpr (st == 1) STAMP(3);
    if constexpr (st == 10) STAMP(6);
    if constexpr (st + G1_NS - 1 < 20) issue(st + G1_NS - 1, (st + G1_NS - 1) % G1_NS);
    const char* bp = sm + (st % G1_NS) * G1_STG + boff;
#pragma unroll
    for (int j = 0; j < 4; ++j) {
      if (j < ncnt) {
        u32x4 bf = *(const u32x4*)(bp + j * 512);
        if constexpr (st < 10) {
          acc[j][0] = mfma_b(af[st][0], bf, acc[j][0]);
          acc[j][1] = mfma_b(af[st][1], bf, acc[j][1]);
        } else {
          acc[j][0] = mfma_b(bf, af[st - 10][0], acc[j][0]);
          acc[j][1] = mfma_b(bf, af[st - 10][1], acc[j][1]);
        }
      }
    }
    if constexpr (st == 9) {
      STAMP(4);
      reduce();
      STAMP(5);
      float zv[4][4], sum[4] = {0.f, 0.f, 0.f, 0.f};
#pragma unroll
      for (int j = 0; j < 4; ++j) {
        u32 u0 = pk_bf(acc[j][0][0], acc[j][0][1]), u1 = pk_bf(acc[j][0][2], acc[j][0][3]);
        z2p[j][0] = pk_bf(gelu_f(bf_lo(u0)), gelu_f(bf_hi(u0)));
        z2p[j][1] = pk_bf(gelu_f(bf_lo(u1)), gelu_f(bf_hi(u1)));
        zv[j][0] = bf_lo(z2p[j][0]); zv[j][1] = bf_hi(z2p[j][0]);
        zv[j][2] = bf_lo(z2p[j][1]); zv[j][3] = bf_hi(z2p[j][1]);
#pragma unroll
        for (int r = 0; r < 4; ++r) sum[r] += (j < ncnt) ? zv[j][r] : 0.f;
      }
      const float inv = 1.0f / (float)(ncnt * 16);
      float mean[4], m2[4];
#pragma unroll
      for (int r = 0; r < 4; ++r) {
        mean[r] = row16_sum(sum[r]) * inv;
        float q = 0.f;
#pragma unroll
        for (int j = 0; j < 4; ++j) { float d = zv[j][r] - mean[r]; q += (j < ncnt) ? d * d : 0.f; }
        m2[r] = row16_sum(q);
      }
      float mm = n == 0 ? mean[0] : n == 1 ? mean[1] : n == 2 ? mean[2] : mean[3];
      float qq = n == 0 ? m2[0] : n == 1 ? m2[1] : n == 2 ? m2[2] : m2[3];
      if (n < 4) {
        u64 gr = (u64)__builtin_bit_cast(u32, mm) | ((u64)(__builtin_bit_cast(u32, qq) & 0x7fffffffu) << 32);
        __hip_atomic_store((GLB_AS u64*)(ws + WS_STAT) + (size_t)(srow + 4 * g + n) * 16 + cg * 2 + np, gr,
                           __ATOMIC_RELAXED, __HIP_MEMORY_SCOPE_AGENT);
      }
    }
  });
  STAMP(7);
  wait_lgkm0(); raw_barrier();
  reduce();
  STAMP(8);
  {
    u32x2* z1 = (u32x2*)(ws + WS_Z1);
#pragma unroll
    for (int j = 0; j < 4; ++j) {
      if (j < ncnt) {
        u32 u0 = pk_bf(acc[j][0][0], acc[j][0][1]), u1 = pk_bf(acc[j][0][2], acc[j][0][3]);
        u32x2 o = {pk_bf(gelu_f(bf_lo(u0)), gelu_f(bf_hi(u0))), pk_bf(gelu_f(bf_lo(u1)), gelu_f(bf_hi(u1)))};
        z1[((size_t)(t0 + nfirst + j) * 2048 + srow + n) * 4 + g] = o;
      }
    }
  }
  {
    const GLB_AS u64* sp = (const GLB_AS u64*)(ws + WS_STAT) + (size_t)(srow + 4 * g) * 16 + n;
    u64 gr[4];
    for (int spin = 0; spin < (1 << 22); ++spin) {
      bool ok = true;
#pragma unroll
      for (int r = 0; r < 4; ++r) {
        gr[r] = __hip_atomic_load(sp + r * 16, __ATOMIC_RELAXED, __HIP_MEMORY_SCOPE_AGENT);
        ok = ok && !(gr[r] >> 63);
      }
      if (__all(ok)) break;
      __builtin_amdgcn_s_sleep(1);
    }
    STAMP(9);
    const float cnt = (n & 1) ? 48.f : (((n >> 1) & 3) == 3 ? 48.f : 64.f);
    f2 ln[4];
#pragma unroll
    for (int r = 0; r < 4; ++r) {
      float mp = __builtin_bit_cast(float, (u32)gr[r]), qp = __builtin_bit_cast(float, (u32)(gr[r] >> 32));
      float mean = row16_sum(mp * cnt) * (1.0f / 864.0f);
      float d = mp - mean;
      float var = row16_sum(qp + cnt * d * d) * (1.0f / 864.0f);
      ln[r] = ln_scalars(mean, var);
    }
    u32x2* z2 = (u32x2*)(ws + WS_Z2);
#pragma unroll
    for (int j = 0; j < 4; ++j) {
      if (j < ncnt) {
        u32 d0 = pk_bf(bf_lo(z2p[j][0]) - ln[0].x, bf_hi(z2p[j][0]) - ln[1].x);
        u32 d1 = pk_bf(bf_lo(z2p[j][1]) - ln[2].x, bf_hi(z2p[j][1]) - ln[3].x);
        u32x2 o = {pk_bf(bf_lo(d0) * ln[0].y, bf_hi(d0) * ln[1].y), pk_bf(bf_lo(d1) * ln[2].y, bf_hi(d1) * ln[3].y)};
        z2[((size_t)(srow >> 4) * FH + (t0 + nfirst + j) * 16 + n) * 4 + g] = o;
      }
    }
  }
  STAMP(10);
  STAMP_OUT(3);
}

constexpr int G2_NS = 7, G2_A = 14336, G2_STG = 14336 + 8192, G2_LDS = G2_NS * G2_STG;
__global__ __launch_bounds__(512, 2) void k5_gemm2(const char* __restrict__ wsp, const u16* __restrict__ bsp,
                                                   char* __restrict__ ws) {
  extern __shared__ __attribute__((aligned(16))) char sm[];
  const int tid = threadIdx.x, lane = tid & 63, w = uni(tid >> 6);
  const int n = lane & 15, g = lane >> 4;
  int rt, cg;
  tile_map(rt, cg);
  const int nt_all = cg_count(cg), t0 = cg_start(cg);
  const int kg = w >> 2, mh = (w >> 1) & 1, np = (w ^ (w >> 2)) & 1;
  const int nfirst = np ? nt_all - 3 : 0, ncnt = np ? 3 : nt_all - 3;
  const int rows = nt_all * 16, per = rows >> 1;
  STAMP_DECL; STAMP(0);
  const char* asrc[2];
#pragma unroll
  for (int i = 0; i < 2; ++i) {
    int c = (2 * w + i) * per + (lane < per ? lane : 0);
    int kb = c / (2 * rows), rem = c - kb * 2 * rows;
    asrc[i] = ws + WS_Z2 + ((size_t)kb * FH + t0 * 16) * 32 + rem * 16;
  }
  const int brow = w * 8 + (lane >> 3);
  const char* bsrc = wsp + (size_t)(rt * 64 + brow) * 4096 + (((lane & 7) ^ ((brow >> 1) & 7)) << 4);
  const int trow = rt * 64 + (2 * mh + kg) * 16 + n;
  u32x2 z1v[4];
#pragma unroll
  for (int j = 0; j < 4; ++j) {
    int jj = j < ncnt ? j : 0;
    z1v[j] = ((const u32x2*)(ws + WS_Z1))[((size_t)(t0 + nfirst + jj) * 2048 + trow) * 4 + g];
  }
  const float bias = bf_lo(bsp[trow]);
  auto issue = [&](int st, int slot) {
    char* base = sm + slot * G2_STG;
    glds16(bsrc + st * 128, base + G2_A + w * 1024);
    if (lane < per) {
      glds16(asrc[0] + (size_t)st * (4 * FH * 32), base + (2 * w) * per * 16);
      glds16(asrc[1] + (size_t)st * (4 * FH * 32), base + (2 * w + 1) * per * 16);
    }
  };
#pragma unroll
  for (int st = 0; st < G2_NS - 1; ++st) issue(st, st);
  const int aoff = (2 * kg + (g >> 1)) * rows * 32 + (nfirst * 16 + n) * 32 + (g & 1) * 16;
  int boff[2];
#pragma unroll
  for (int m = 0; m < 2; ++m) {
    int r = mh * 32 + m * 16 + n;
    boff[m] = G2_A + r * 128 + (((4 * kg + g) ^ ((r >> 1) & 7)) << 4);
  }
  f4 acc[4][2];
#pragma unroll
  for (int j = 0; j < 4; ++j) { acc[j][0] = f4{0.f, 0.f, 0.f, 0.f}; acc[j][1] = f4{0.f, 0.f, 0.f, 0.f}; }
  auto comp = [&](int slot) {
    const char* base = sm + slot * G2_STG;
    u32x4 b0 = *(const u32x4*)(base + boff[0]), b1 = *(const u32x4*)(base + boff[1]);
#pragma unroll
    for (int j = 0; j < 4; ++j) {
      if (j < ncnt) {
        u32x4 a = *(const u32x4*)(base + aoff + j * 512);
        acc[j][0] = mfma_b(a, b0, acc[j][0]);
        acc[j][1] = mfma_b(a, b1, acc[j][1]);
      }
    }
  };
  int cur = 0, nxt = G2_NS - 1;
  STAMP(1);
  for (int st = 0; st < 32 - (G2_NS - 1); ++st) {
    wait_vm<3 * (G2_NS - 2)>(); wait_lgkm0(); raw_barrier();
    issue(st + G2_NS - 1, nxt);
    comp(cur);
    cur = cur + 1 == G2_NS ? 0 : cur + 1;
    nxt = nxt + 1 == G2_NS ? 0 : nxt + 1;
  }
  static_for<0, G2_NS - 1>([&](auto I) {
    wait_vm<3 * (G2_NS - 2 - I.value)>(); wait_lgkm0(); raw_barrier();
    comp(cur);
    cur = cur + 1 == G2_NS ? 0 : cur + 1;
  });
  STAMP(6);
  wait_lgkm0(); raw_barrier();
  f4* red = (f4*)sm;
#pragma unroll
  for (int j = 0; j < 4; ++j) red[(w * 4 + j) * 64 + lane] = sel4(kg, acc[j][0], acc[j][1]);
  wait_lgkm0(); raw_barrier();
  const int pw = (w ^ 4) ^ 1;
  u32x2* P = (u32x2*)(ws + WS_P);
#pragma unroll
  for (int j = 0; j < 4; ++j) {
    if (j < ncnt) {
      f4 v = sel4(kg, acc[j][1], acc[j][0]) + red[(pw * 4 + j) * 64 + lane];
      u32 g0 = pk_bf(v[0], v[1]), g1 = pk_bf(v[2], v[3]);
      g0 = pk_bf(bf_lo(g0) + bias, bf_hi(g0) + bias);
      g1 = pk_bf(bf_lo(g1) + bias, bf_hi(g1) + bias);
      u32x2 o = {pk_bf(bf_lo(g0) * bf_lo(z1v[j][0]), bf_hi(g0) * bf_hi(z1v[j][0])),
                 pk_bf(bf_lo(g1) * bf_lo(z1v[j][1]), bf_hi(g1) * bf_hi(z1v[j][1]))};
      P[((size_t)(t0 + nfirst + j) * 2048 + trow) * 4 + g] = o;
    }
  }
  STAMP(7);
  STAMP_OUT(4);
}

constexpr int G3_NS = 8, G3_A = 10240, G3_STG = 10240 + 8192, G3_LDS = G3_NS * G3_STG;
__global__ __launch_bounds__(512, 2) void k6_gemm3(char* __restrict__ ws, u32x2* __restrict__ out) {
  extern __shared__ __attribute__((aligned(16))) char sm[];
  const int tid = threadIdx.x, lane = tid & 63, w = uni(tid >> 6);
  const int n = lane & 15, g = lane >> 4;
  int rt, cg;
  tile_map(rt, cg);
  const int kg = w >> 2, mh = (w >> 1) & 1, np = (w ^ (w >> 2)) & 1;
  const int nfirst = np ? 3 : 0, ncnt = np ? 2 : 3;
  STAMP_DECL; STAMP(0);
  const int akb = w >> 1;
  const char* asrc = ws + WS_WOUT + ((size_t)cg * 80) * 32 + ((w & 1) * 80 + (lane < 40 ? lane : 0)) * 16;
  const char* bsrc = ws + WS_P + ((size_t)rt * 64) * 32 + ((w & 1) * 64 + lane) * 16;
  const int trow = rt * 64 + (2 * mh + kg) * 16 + n;
  u32x2 hv[3];
#pragma unroll
  for (int j = 0; j < 3; ++j) {
    int jj = j < ncnt ? j : 0;
    hv[j] = ((const u32x2*)(ws + WS_H))[((size_t)(cg * 5 + nfirst + jj) * 2048 + trow) * 4 + g];
  }
  auto issue = [&](int st, int slot) {
    char* base = sm + slot * G3_STG;
    int kb = st * 4 + akb;
    kb = kb < 54 ? kb : 53;
    glds16(bsrc + (size_t)kb * (2048 * 32), base + G3_A + w * 1024);
    if (lane < 40) {
      glds16(asrc + (size_t)kb * (D * 32), base + w * 1280);
      glds16(asrc + (size_t)kb * (D * 32) + 640, base + w * 1280 + 640);
    }
  };
#pragma unroll
  for (int st = 0; st < G3_NS - 1; ++st) issue(st, st);
  const int aoff = (2 * kg + (g >> 1)) * 2560 + (nfirst * 16 + n) * 32 + (g & 1) * 16;
  const int boff = G3_A + (2 * kg + (g >> 1)) * 2048 + (mh * 32 + n) * 32 + (g & 1) * 16;
  f4 acc[3][2];
#pragma unroll
  for (int j = 0; j < 3; ++j) { acc[j][0] = f4{0.f, 0.f, 0.f, 0.f}; acc[j][1] = f4{0.f, 0.f, 0.f, 0.f}; }
  static_for<0, 14>([&](auto ST) {
    constexpr int st = ST.value;
    constexpr int left = (13 - st) < (G3_NS - 2) ? (13 - st) : (G3_NS - 2);
    if constexpr (st == 0) STAMP(1);
    wait_vm<3 * left>(); wait_lgkm0(); raw_barrier();
    if constexpr (st == 0) STAMP(2);
    if constexpr (st == 7) STAMP(3);
    if constexpr (st + G3_NS - 1 < 14) issue(st + G3_NS - 1, (st + G3_NS - 1) % G3_NS);
    if (st < 13 || kg == 0) {
      const char* base = sm + (st % G3_NS) * G3_STG;
      u32x4 b0 = *(const u32x4*)(base + boff), b1 = *(const u32x4*)(base + boff + 512);
#pragma unroll
      for (int j = 0; j < 3; ++j) {
        if (j < ncnt) {
          u32x4 a = *(const u32x4*)(base + aoff + j * 512);
          acc[j][0] = mfma_b(a, b0, acc[j][0]);
          acc[j][1] = mfma_b(a, b1, acc[j][1]);
        }
      }
    }
  });
  STAMP(6);
  wait_lgkm0(); raw_barrier();
  f4* red = (f4*)sm;
#pragma unroll
  for (int j = 0; j < 3; ++j) red[(w * 4 + j) * 64 + lane] = sel4(kg, acc[j][0], acc[j][1]);
  wait_lgkm0(); raw_barrier();
  const int pw = (w ^ 4) ^ 1;
#pragma unroll
  for (int j = 0; j < 3; ++j) {
    if (j < ncnt) {
      f4 v = sel4(kg, acc[j][1], acc[j][0]) + red[(pw * 4 + j) * 64 + lane];
      u32 o0 = pk_bf(v[0], v[1]), o1 = pk_bf(v[2], v[3]);
      u32x2 o = {pk_bf(bf_lo(hv[j][0]) + bf_lo(o0), bf_hi(hv[j][0]) + bf_hi(o0)),
                 pk_bf(bf_lo(hv[j][1]) + bf_lo(o1), bf_hi(hv[j][1]) + bf_hi(o1))};
      out[(size_t)trow * 160 + (cg * 5 + nfirst + j) * 4 + g] = o;
    }
  }
  STAMP(7);
  STAMP_OUT(5);
}

extern "C" void kernel_launch(void* const* d_in, const int* in_sizes, int n_in, void* d_out, int out_size,
                              void* d_ws, size_t ws_size, hipStream_t stream) {
  (void)in_sizes; (void)n_in; (void)out_size; (void)ws_size;
  char* ws = (char*)d_ws;
  (void)hipFuncSetAttribute((const void*)k4_gemm1, hipFuncAttributeMaxDynamicSharedMemorySize, G1_LDS);
  (void)hipFuncSetAttribute((const void*)k5_gemm2, hipFuncAttributeMaxDynamicSharedMemorySize, G2_LDS);
  (void)hipFuncSetAttribute((const void*)k6_gemm3, hipFuncAttributeMaxDynamicSharedMemorySize, G3_LDS);
  k1_fwd<<<256 + 64, 512, K1_LDS + 256, stream>>>((const u32*)d_in[0], (const u32x4*)d_in[1], (const u32x4*)d_in[2], ws);
  k2_col<<<KT + K2_NWIN + K2_NWOUT + K2_NSTAT + 128, 512, K2_LDS, stream>>>((const u32*)d_in[4], (const u32*)d_in[7], ws, (const char*)d_in[5]);
  k3_inv<<<256, 512, K3_LDS, stream>>>((const u32x2*)d_in[0], ws);
  k4_gemm1<<<256, 512, G1_LDS, stream>>>(ws);
  k5_gemm2<<<256, 512, G2_LDS, stream>>>((const char*)d_in[5], (const u16*)d_in[6], ws);
  k6_gemm3<<<256, 512, G3_LDS, stream>>>(ws, (u32x2*)d_out);
}
```

```cpp
#include <hip/hip_runtime.h>
#include <stdint.h>
#include <type_traits>

typedef unsigned int u32;
typedef unsigned short u16;
typedef unsigned long long u64;
typedef _Float16 h2 __attribute__((ext_vector_type(2)));
typedef _Float16 h4 __attribute__((ext_vector_type(4)));
typedef _Float16 h8 __attribute__((ext_vector_type(8)));
typedef short s4 __attribute__((ext_vector_type(4)));
typedef __bf16 b2 __attribute__((ext_vector_type(2)));
typedef __bf16 b8 __attribute__((ext_vector_type(8)));
typedef float f2 __attribute__((ext_vector_type(2)));
typedef float f4 __attribute__((ext_vector_type(4)));
typedef u32 u32x2 __attribute__((ext_vector_type(2)));
typedef u32 u32x4 __attribute__((ext_vector_type(4)));
#define DI __device__ __forceinline__
#define LDS_AS __attribute__((address_space(3)))
#define GLB_AS __attribute__((address_space(1)))

constexpr int S = 2048, D = 640, FH = 864, F = 1728, KT = 321;

constexpr size_t MB = 1u << 20;
constexpr size_t WS_T1 = 0 * MB;
constexpr size_t WS_T2 = 3 * MB;
constexpr size_t WS_WT = 6 * MB;
constexpr size_t WS_H = 9 * MB;
constexpr size_t WS_HN = 12 * MB;
constexpr size_t WS_WIN = 15 * MB;
constexpr size_t WS_WOUT = 18 * MB;
constexpr size_t WS_Z2 = 20 * MB;
constexpr size_t WS_P = 24 * MB;
constexpr size_t WS_STAT = 28 * MB;
constexpr size_t WS_FLAG = WS_STAT + 262144;
constexpr size_t WS_WMAX = WS_FLAG + 16384;
constexpr size_t WS_END = 29 * MB;

constexpr double kPi = 3.14159265358979323846264338327950288;
constexpr double c_sin_small(double x) {
  double t = x, s = x;
  for (int i = 1; i < 10; ++i) { t *= -x * x / ((2 * i) * (2 * i + 1)); s += t; }
  return s;
}
constexpr double c_cos_small(double x) {
  double t = 1, s = 1;
  for (int i = 1; i < 10; ++i) { t *= -x * x / ((2 * i - 1) * (2 * i)); s += t; }
  return s;
}
struct Cos640 { double c[640]; };
constexpr Cos640 make_cos640() {
  Cos640 t{};
  for (int j = 0; j <= 160; ++j) {
    double v = (j <= 80) ? c_cos_small(2 * kPi * j / 640) : c_sin_small(2 * kPi * (160 - j) / 640);
    if (j == 160) v = 0.0;
    t.c[j] = v;
    t.c[(640 - j) % 640] = v;
    t.c[320 - j] = -v;
    t.c[(320 + j) % 640] = -v;
  }
  return t;
}
constexpr Cos640 kCos = make_cos640();
constexpr double cs(int j) { return kCos.c[((j % 640) + 640) % 640]; }
constexpr double sn(int j) { return kCos.c[(((j - 160) % 640) + 640) % 640]; }
constexpr u32 pkh(double lo, double hi) {
  return (u32)__builtin_bit_cast(u16, (_Float16)(float)lo) | ((u32)__builtin_bit_cast(u16, (_Float16)(float)hi) << 16);
}
constexpr u32 fb(double v) { return __builtin_bit_cast(u32, (float)v); }
constexpr double kRs40 = 0.15811388300841896659994467722163593;

struct Tab { u32 v[12][64][4]; };
constexpr Tab make_tab_fwd() {
  Tab t{};
  for (int l = 0; l < 64; ++l) {
    int n = l & 15, g = l >> 4;
    for (int nt = 0; nt < 2; ++nt) {
      int k1 = 16 * nt + n;
      bool ok = k1 <= 20;
      for (int q = 0; q < 4; ++q) {
        int a0 = (q < 2) ? 4 * g + 2 * q : 16 + 4 * g + 2 * (q - 2), a1 = a0 + 1;
        t.v[0 + nt][l][q] = ok ? pkh(cs(16 * a0 * k1), cs(16 * a1 * k1)) : 0u;
        t.v[2 + nt][l][q] = ok ? pkh(-sn(16 * a0 * k1), -sn(16 * a1 * k1)) : 0u;
      }
      for (int q = 0; q < 2; ++q) {
        int a0 = 32 + 4 * g + 2 * q, a1 = a0 + 1;
        bool v = ok && a0 < 40;
        t.v[4][l][2 * nt + q] = v ? pkh(cs(16 * a0 * k1), cs(16 * a1 * k1)) : 0u;
        t.v[5][l][2 * nt + q] = v ? pkh(-sn(16 * a0 * k1), -sn(16 * a1 * k1)) : 0u;
      }
      for (int r = 0; r < 4; ++r) {
        int b = 4 * g + r;
        t.v[6 + 2 * nt][l][r] = fb(cs(b * k1) * kRs40);
        t.v[7 + 2 * nt][l][r] = fb(-sn(b * k1) * kRs40);
      }
    }
    for (int q = 0; q < 4; ++q) {
      int b = 4 * g + q, k2 = n;
      double c = cs(40 * b * k2) * 0.25, s = sn(40 * b * k2) * 0.25, sg = (k2 >= 8) ? -1.0 : 1.0;
      t.v[10][l][q] = pkh(c, s);
      t.v[11][l][q] = pkh(-s * sg, c * sg);
    }
  }
  return t;
}
constexpr Tab make_tab_inv() {
  Tab t{};
  for (int l = 0; l < 64; ++l) {
    int n = l & 15, g = l >> 4;
    for (int q = 0; q < 4; ++q) {
      int k2 = 4 * g + q, b = n;
      double c = cs(40 * b * k2) * 0.25, s = sn(40 * b * k2) * 0.25, sg = (k2 >= 8) ? -1.0 : 1.0;
      t.v[0][l][q] = pkh(c, -s * sg);
      t.v[1][l][q] = pkh(s, c * sg);
    }
    for (int mt = 0; mt < 2; ++mt) {
      for (int r = 0; r < 4; ++r) {
        int k1 = 16 * mt + 4 * g + r, b = n;
        t.v[2 + 2 * mt][l][r] = fb(cs(b * k1) * kRs40);
        t.v[3 + 2 * mt][l][r] = fb(sn(b * k1) * kRs40);
      }
      for (int nt = 0; nt < 3; ++nt) {
        int a = 16 * nt + n;
        for (int q = 0; q < 4; ++q) {
          int k1 = 16 * mt + 4 * g + q;
          double w = (k1 == 0 || k1 == 20) ? 1.0 : 2.0;
          bool ok = k1 <= 20 && a < 40;
          t.v[6 + 2 * nt + mt][l][q] = ok ? pkh(w * cs(16 * a * k1), -w * sn(16 * a * k1)) : 0u;
        }
      }
    }
  }
  return t;
}
static __device__ constexpr Tab g_tab_fwd = make_tab_fwd();
static __device__ constexpr Tab g_tab_inv = make_tab_inv();

#define STAMP_DECL do {} while (0)
#define STAMP(I) do {} while (0)
#define STAMP_OUT(K) do {} while (0)
#define SB() __builtin_amdgcn_sched_barrier(0)
template <int V> using IC = std::integral_constant<int, V>;
template <int I, int N, class Fn> DI void static_for(Fn&& f) {
  if constexpr (I < N) { f(IC<I>{}); static_for<I + 1, N>(f); }
}
DI float as_f(u32 v) { return __builtin_bit_cast(float, v); }
DI u32 as_u(float v) { return __builtin_bit_cast(u32, v); }
DI float bf_lo(u32 v) { return __builtin_bit_cast(float, v << 16); }
DI float bf_hi(u32 v) { return __builtin_bit_cast(float, v & 0xffff0000u); }
DI u32 pk_bf(float a, float b) { f2 x = {a, b}; return __builtin_bit_cast(u32, __builtin_convertvector(x, b2)); }
DI u32 pk_h(float a, float b) { f2 x = {a, b}; return __builtin_bit_cast(u32, __builtin_convertvector(x, h2)); }
DI float rbf(float a) { return bf_lo(pk_bf(a, a)); }
DI float opq(float v) { asm("" : "+v"(v)); return v; }
template <int CTRL> DI u32 dppu(u32 v) { return (u32)__builtin_amdgcn_update_dpp(0, (int)v, CTRL, 0xf, 0xf, false); }
template <int CTRL> DI float dpp(float v) { return __builtin_bit_cast(float, dppu<CTRL>(__builtin_bit_cast(u32, v))); }
DI float row16_sum(float v) {
  v += dpp<0x128>(v); v += dpp<0x124>(v); v += dpp<0x122>(v); v += dpp<0x121>(v);
  return v;
}
DI float wave_sum(float v) {
  v = row16_sum(v);
  u32 u = __builtin_bit_cast(u32, v);
  auto a = __builtin_amdgcn_permlane16_swap(u, u, false, false);
  v = opq(__builtin_bit_cast(float, (u32)a[0])) + opq(__builtin_bit_cast(float, (u32)a[1]));
  u = __builtin_bit_cast(u32, v);
  auto b = __builtin_amdgcn_permlane32_swap(u, u, false, false);
  return opq(__builtin_bit_cast(float, (u32)b[0])) + opq(__builtin_bit_cast(float, (u32)b[1]));
}
DI u32 umax(u32 a, u32 b) { return a > b ? a : b; }
DI u32 wave_umax(u32 v) {
  v = umax(v, dppu<0x128>(v)); v = umax(v, dppu<0x124>(v)); v = umax(v, dppu<0x122>(v)); v = umax(v, dppu<0x121>(v));
  auto a = __builtin_amdgcn_permlane16_swap(v, v, false, false);
  v = umax(a[0], a[1]);
  auto b = __builtin_amdgcn_permlane32_swap(v, v, false, false);
  return umax(b[0], b[1]);
}
DI u32x4 widen(u32x2 a, u32x2 b) {
  auto x = __builtin_amdgcn_permlane16_swap(a[0], b[0], false, false);
  auto y = __builtin_amdgcn_permlane16_swap(a[1], b[1], false, false);
  return u32x4{x[0], y[0], x[1], y[1]};
}
DI u32x4 cmul_pk(f4 re, f4 im, f4 tx, f4 ty) {
  f4 a = re * tx - im * ty, b = re * ty + im * tx;
  return u32x4{pk_h(a[0], b[0]), pk_h(a[1], b[1]), pk_h(a[2], b[2]), pk_h(a[3], b[3])};
}
DI float h_lo(u32 v) { return (float)__builtin_bit_cast(h2, v)[0]; }
DI float h_hi(u32 v) { return (float)__builtin_bit_cast(h2, v)[1]; }
template <int SG> DI void radix8_half(int e, f4 ar, f4 ai, f4 br, f4 bi, f4& xr, f4& xi) {
  f4 pr, pi;
  if (e == 0) {
    pr = ar + br; pi = ai + bi;
  } else {
    const float r2 = 0.70710678118654752f;
    const f4 cx = {1.f, r2, 0.f, -r2}, cy = {0.f, SG * r2, SG * 1.f, SG * r2};
    f4 dr = ar - br, di = ai - bi;
    pr = dr * cx - di * cy; pi = dr * cy + di * cx;
  }
  f2 sr = pr.xy + pr.zw, si = pi.xy + pi.zw;
  f2 dr = pr.xy - pr.zw, di = pi.xy - pi.zw;
  xr[0] = opq(sr.x + sr.y); xi[0] = opq(si.x + si.y);
  xr[2] = opq(sr.x - sr.y); xi[2] = opq(si.x - si.y);
  if (SG < 0) {
    xr[1] = opq(dr.x + di.y); xi[1] = opq(di.x - dr.y);
    xr[3] = opq(dr.x - di.y); xi[3] = opq(di.x + dr.y);
  } else {
    xr[1] = opq(dr.x - di.y); xi[1] = opq(di.x + dr.y);
    xr[3] = opq(dr.x + di.y); xi[3] = opq(di.x - dr.y);
  }
}
DI f4 mfma_h(u32x4 a, u32x4 b, f4 c) {
  return __builtin_amdgcn_mfma_f32_16x16x32_f16(__builtin_bit_cast(h8, a), __builtin_bit_cast(h8, b), c, 0, 0, 0);
}
DI f4 mfma_b(u32x4 a, u32x4 b, f4 c) {
  return __builtin_amdgcn_mfma_f32_16x16x32_bf16(__builtin_bit_cast(b8, a), __builtin_bit_cast(b8, b), c, 0, 0, 0);
}
DI u32x2 tr_read(const char* p) {
  return __builtin_bit_cast(u32x2, __builtin_amdgcn_ds_read_tr16_b64_v4i16((LDS_AS s4*)p));
}
DI void glds(const void* sbase, u32 voff, u32 lds) {
  asm volatile("s_mov_b32 m0, %2\n\ts_nop 0\n\tglobal_load_lds_dwordx4 %0, %1" ::"v"(voff), "s"(sbase), "s"(lds) : "memory");
}
template <int LOFF, bool SC1> DI void bdma0(u32x4 srd, u32 voff, u32 soff, u32 lds) {
  if (SC1) asm volatile("s_add_i32 m0, %3, %4\n\ts_nop 0\n\tbuffer_load_dwordx4 %0, %1, %2 offen sc1 lds"
                        ::"v"(voff), "s"(srd), "s"(soff), "s"(lds), "n"(LOFF) : "memory", "scc");
  else asm volatile("s_add_i32 m0, %3, %4\n\ts_nop 0\n\tbuffer_load_dwordx4 %0, %1, %2 offen lds"
                    ::"v"(voff), "s"(srd), "s"(soff), "s"(lds), "n"(LOFF) : "memory", "scc");
}
template <bool SC1> DI void bdma1(u32x4 srd, u32 voff, u32 soff) {
  if (SC1) asm volatile("buffer_load_dwordx4 %0, %1, %2 offen offset:1024 sc1 lds" ::"v"(voff), "s"(srd), "s"(soff) : "memory");
  else asm volatile("buffer_load_dwordx4 %0, %1, %2 offen offset:1024 lds" ::"v"(voff), "s"(srd), "s"(soff) : "memory");
}
template <int OFF> DI void ld16_async(u32x4& d, u32x4 srd, u32 voff, u32 soff) {
  asm volatile("buffer_load_dwordx4 %0, %1, %2, %3 offen offset:%4 sc1" : "=v"(d) : "v"(voff), "s"(srd), "s"(soff), "n"(OFF) : "memory");
}
template <int N> DI void wait_vm_tie(u32x4& d) { asm volatile("s_waitcnt vmcnt(%1)" : "+v"(d) : "n"(N) : "memory"); }
template <int N> DI void wait_lgkm() { __builtin_amdgcn_s_waitcnt(0xC07F | (N << 8)); SB(); }
DI u32x4 lds_rd(u32 a) { return *(const LDS_AS u32x4*)(size_t)a; }
template <int N> DI void wait_vm() {
  asm volatile("s_waitcnt vmcnt(%0)" ::"n"(N) : "memory");
  if (N == 0) __builtin_amdgcn_s_waitcnt(0x0F70);
}
DI void lgkm0() { asm volatile("" ::: "memory"); __builtin_amdgcn_s_waitcnt(0xC07F); asm volatile("" ::: "memory"); }
DI void bar() { asm volatile("" ::: "memory"); __builtin_amdgcn_s_barrier(); asm volatile("" ::: "memory"); }
DI int uni(int v) { return __builtin_amdgcn_readfirstlane(v); }
DI u32 lds_addr(const char* p) { return (u32)(size_t)(const LDS_AS char*)p; }

DI f2 ln_scalars(float mean, float var) {
  float m = rbf(mean);
  float v = rbf(var);
  float den = rbf(__builtin_amdgcn_sqrtf(rbf(v + 1e-5f)));
  return f2{m, __builtin_amdgcn_rcpf(den)};
}
DI u32 ln_pair(u32 v, float m0, float r0, float m1, float r1) {
  u32 d = pk_bf(bf_lo(v) - m0, bf_hi(v) - m1);
  return pk_bf(bf_lo(d) * r0, bf_hi(d) * r1);
}
DI f2 gelu2f(u32 p) {
  const float c1 = -2.0f * 0.7978845608028654f * 1.4426950408889634f, c2 = c1 * 0.044715f;
  const f2 u = {bf_lo(p), bf_hi(p)};
  const f2 t = u * (u * u * f2{c2, c2} + f2{c1, c1});
  const f2 d = f2{__builtin_amdgcn_exp2f(t.x), __builtin_amdgcn_exp2f(t.y)} + f2{1.f, 1.f};
  return u * f2{__builtin_amdgcn_rcpf(d.x), __builtin_amdgcn_rcpf(d.y)};
}
DI u32 gelu2(u32 p) { f2 o = gelu2f(p); return pk_bf(o.x, o.y); }
DI u32x2 gelu4(f4 v) { return u32x2{gelu2(pk_bf(v[0], v[1])), gelu2(pk_bf(v[2], v[3]))}; }
DI f2 row16_reduce_scatter(f4 a, f4 b) {
  float a01, a23, b01, b23, ra, rb;
  asm("s_nop 1\n\t"
      "v_add_f32_dpp %0, %6, %6 row_ror:8 row_mask:0xf bank_mask:0x3\n\t"
      "v_add_f32_dpp %1, %7, %7 row_ror:8 row_mask:0xf bank_mask:0x3\n\t"
      "v_add_f32_dpp %2, %10, %10 row_ror:8 row_mask:0xf bank_mask:0x3\n\t"
      "v_add_f32_dpp %3, %11, %11 row_ror:8 row_mask:0xf bank_mask:0x3\n\t"
      "v_add_f32_dpp %0, %8, %8 row_ror:8 row_mask:0xf bank_mask:0xc\n\t"
      "v_add_f32_dpp %1, %9, %9 row_ror:8 row_mask:0xf bank_mask:0xc\n\t"
      "v_add_f32_dpp %2, %12, %12 row_ror:8 row_mask:0xf bank_mask:0xc\n\t"
      "v_add_f32_dpp %3, %13, %13 row_ror:8 row_mask:0xf bank_mask:0xc\n\t"
      "v_add_f32_dpp %4, %0, %0 row_half_mirror row_mask:0xf bank_mask:0x5\n\t"
      "v_add_f32_dpp %4, %1, %1 row_half_mirror row_mask:0xf bank_mask:0xa\n\t"
      "v_add_f32_dpp %5, %2, %2 row_half_mirror row_mask:0xf bank_mask:0x5\n\t"
      "v_add_f32_dpp %5, %3, %3 row_half_mirror row_mask:0xf bank_mask:0xa\n\t"
      "s_nop 0\n\t"
      "v_add_f32_dpp %4, %4, %4 quad_perm:[1,0,3,2] row_mask:0xf bank_mask:0xf\n\t"
      "v_add_f32_dpp %5, %5, %5 quad_perm:[1,0,3,2] row_mask:0xf bank_mask:0xf\n\t"
      "s_nop 0\n\t"
      "v_add_f32_dpp %4, %4, %4 quad_perm:[2,3,0,1] row_mask:0xf bank_mask:0xf\n\t"
      "v_add_f32_dpp %5, %5, %5 quad_perm:[2,3,0,1] row_mask:0xf bank_mask:0xf"
      : "=&v"(a01), "=&v"(a23), "=&v"(b01), "=&v"(b23), "=&v"(ra), "=&v"(rb)
      : "v"(a[0]), "v"(a[1]), "v"(a[2]), "v"(a[3]), "v"(b[0]), "v"(b[1]), "v"(b[2]), "v"(b[3]));
  return f2{ra, rb};
}

constexpr int K1_TAB = 0, K1_XN = 12288, K1_STG = 24576, K1_LDS = 24576 + 8 * 328 * 4;
__global__ __launch_bounds__(512) void k1_fwd(const u32* __restrict__ x, const u32x4* __restrict__ wr,
                                              const u32x4* __restrict__ wi, char* __restrict__ ws) {
  extern __shared__ __attribute__((aligned(16))) char sm[];
  const int tid = threadIdx.x, lane = tid & 63, w = uni(tid >> 6);
  const int n = lane & 15, g = lane >> 4;
  STAMP_DECL; STAMP(0);
  const int b = blockIdx.x & 255;
  const int rg = (b & 7) * 32 + (b >> 3);
  if (blockIdx.x >= 256) {
    u32 mx = 0;
    if (tid < KT) {
      u32x4 a = wr[(size_t)rg * KT + tid], c = wi[(size_t)rg * KT + tid];
      *(u32x4*)(sm + tid * 16) = a;
      *(u32x4*)(sm + 5136 + tid * 16) = c;
#pragma unroll
      for (int i = 0; i < 4; ++i) {
        mx = umax(mx, umax(a[i] & 0x7fffu, (a[i] >> 16) & 0x7fffu));
        mx = umax(mx, umax(c[i] & 0x7fffu, (c[i] >> 16) & 0x7fffu));
      }
    }
    mx = wave_umax(mx);
    u32* red = (u32*)(sm + 10272);
    if (lane == 0) red[w] = mx;
    __syncthreads();
    if (tid < KT) {
      const u16* s16 = (const u16*)sm + tid;
      u32x4 o[2];
#pragma unroll
      for (int r = 0; r < 8; ++r) o[r >> 2][r & 3] = (u32)s16[r * KT] | ((u32)s16[2568 + r * KT] << 16);
      u32x4* dst = (u32x4*)(ws + WS_WT) + (size_t)tid * 512 + rg * 2;
      dst[0] = o[0];
      dst[1] = o[1];
    } else if (tid == 448) {
      u32 m = 0;
#pragma unroll
      for (int i = 0; i < 8; ++i) m = umax(m, red[i]);
      ((u32*)(ws + WS_WMAX))[rg] = m;
    }
    STAMP_OUT(0);
    return;
  }
  const int s = rg * 8 + w;
  u32 xv[5];
#pragma unroll
  for (int i = 0; i < 5; ++i) xv[i] = x[(size_t)s * 320 + i * 64 + lane];
  glds(&g_tab_fwd.v[w][0][0], lane * 16, lds_addr(sm) + K1_TAB + w * 1024);
  if (w < 4) glds(&g_tab_fwd.v[8 + w][0][0], lane * 16, lds_addr(sm) + K1_TAB + (8 + w) * 1024);
  STAMP(1);
  wait_vm<0>();
  STAMP(2);
  const float pilot = bf_lo((u32)uni((int)xv[0]));
  f2 s2 = {0.f, 0.f}, q2 = {0.f, 0.f};
#pragma unroll
  for (int i = 0; i < 5; ++i) {
    f2 d = f2{bf_lo(xv[i]), bf_hi(xv[i])} - pilot;
    s2 += d;
    q2 += d * d;
  }
  const float md = wave_sum(opq(s2.x) + opq(s2.y)) * (1.0f / 640.0f);
  const float qd = wave_sum(opq(q2.x) + opq(q2.y)) * (1.0f / 640.0f);
  const f2 ln = ln_scalars(pilot + md, __builtin_fmaxf(qd - md * md, 0.f));
  char* xn = sm + K1_XN + w * 1536;
#pragma unroll
  for (int i = 0; i < 5; ++i) {
    u32 o = ln_pair(xv[i], ln.x, ln.y, ln.x, ln.y);
    *(u32*)(xn + (i * 64 + lane) * 4) = pk_h(bf_lo(o), bf_hi(o));
  }
  *(u32*)(xn + (320 + lane) * 4) = 0u;
  STAMP(3);
  __syncthreads();
  STAMP(4);
  const u32x4* tab = (const u32x4*)(sm + K1_TAB) + lane;
  u32x2 a0 = tr_read(xn + lane * 8), a1 = tr_read(xn + 512 + lane * 8), a2 = tr_read(xn + 1024 + lane * 8);
  u32x4 af = {a0[0], a0[1], a1[0], a1[1]};
  u32x4 tre = tab[4 * 64], tim = tab[5 * 64], fr = tab[10 * 64], fi = tab[11 * 64];
  u32* stg = (u32*)(sm + K1_STG) + w * 328;
  const f4 z = {0.f, 0.f, 0.f, 0.f};
  f4 ore[2], oim[2];
#pragma unroll
  for (int nt = 0; nt < 2; ++nt) {
    f4 re = mfma_h(af, tab[(0 + nt) * 64], z);
    f4 im = mfma_h(af, tab[(2 + nt) * 64], z);
    const u32x4 at = nt ? u32x4{0u, 0u, a2[0], a2[1]} : u32x4{a2[0], a2[1], 0u, 0u};
    re = mfma_h(at, tre, re);
    im = mfma_h(at, tim, im);
    u32x4 fb_ = cmul_pk(re, im, __builtin_bit_cast(f4, tab[(6 + 2 * nt) * 64]), __builtin_bit_cast(f4, tab[(7 + 2 * nt) * 64]));
    ore[nt] = mfma_h(fr, fb_, z);
    oim[nt] = mfma_h(fi, fb_, z);
  }
#pragma unroll
  for (int nt = 0; nt < 2; ++nt) {
    const int k1 = 16 * nt + n;
#pragma unroll
    for (int r = 0; r < 4; ++r) {
      int k2 = 4 * g + r, k = k1 + 40 * k2;
      int kt = g < 2 ? k : 640 - k;
      bool ok = k1 <= 20 && !(k1 == 0 && k2 > 8) && !(k1 == 20 && k2 >= 8);
      stg[ok ? kt : 324] = pk_h(ore[nt][r], oim[nt][r]);
    }
  }
  STAMP(5);
  __syncthreads();
  STAMP(6);
  if (tid < KT) {
    const u32* sp = (const u32*)(sm + K1_STG) + tid;
    u32x4 o0 = {sp[0], sp[328], sp[2 * 328], sp[3 * 328]};
    u32x4 o1 = {sp[4 * 328], sp[5 * 328], sp[6 * 328], sp[7 * 328]};
    u32x4* dst = (u32x4*)(ws + WS_T1) + (size_t)tid * 512 + rg * 2;
    dst[0] = o0;
    dst[1] = o1;
  }
  STAMP(7);
  STAMP_OUT(0);
}

constexpr int K2_W = 10240 + 16384, K2_LDS = K2_W + 8192;
constexpr int K2_NWIN = 68, K2_NWOUT = 34, K2_NSTAT = 9, K2_NPF = 64;
DI void transpose8(const u32x2* __restrict__ src, int stride, u32x4* __restrict__ dst) {
  u32x2 v[8];
#pragma unroll
  for (int i = 0; i < 8; ++i) v[i] = src[(size_t)i * stride];
#pragma unroll
  for (int c = 0; c < 4; ++c) {
    u32x4 o;
#pragma unroll
    for (int i = 0; i < 4; ++i)
      o[i] = __builtin_amdgcn_perm(v[2 * i + 1][c >> 1], v[2 * i][c >> 1], (c & 1) ? 0x07060302u : 0x05040100u);
    dst[c] = o;
  }
}
__global__ __launch_bounds__(512) void k2_col(const u32x2* __restrict__ w_in, const u32x2* __restrict__ w_out,
                                              const char* __restrict__ wsp, char* __restrict__ ws) {
  extern __shared__ __attribute__((aligned(16))) char sm[];
  const int tid = threadIdx.x, lane = tid & 63, w = uni(tid >> 6);
  const int n = lane & 15, g = lane >> 4;
  STAMP_DECL; STAMP(0);
  if (blockIdx.x >= KT) {
    int q = blockIdx.x - KT;
    if (q < K2_NWIN) {
      int it = q * 512 + tid;
      if (it < 80 * 432) {
        int p = it / 432, c = it - p * 432;
        transpose8(w_in + (size_t)p * 8 * 432 + c, 432, (u32x4*)(ws + WS_WIN) + (size_t)it * 4);
      }
    } else if (q < K2_NWIN + K2_NWOUT) {
      int it = (q - K2_NWIN) * 512 + tid;
      if (it < 108 * 160) {
        int p = it / 160, c = it - p * 160, dg = c / 20;
        transpose8(w_out + (size_t)p * 8 * 160 + c, 160,
                   (u32x4*)(ws + WS_WOUT) + (size_t)(dg * 108 + p) * 80 + (c - dg * 20) * 4);
      }
    } else if (q >= K2_NWIN + K2_NWOUT + K2_NSTAT) {
      const char* p = wsp + (size_t)(q - K2_NWIN - K2_NWOUT - K2_NSTAT) * 131072;
      asm volatile("s_mov_b32 m0, %2\n\ts_nop 0\n\tglobal_load_lds_dword %0, %1\n\tglobal_load_lds_dword %3, %1\n\ts_waitcnt vmcnt(0)"
                   ::"v"(tid * 128), "s"(p), "s"(lds_addr(sm) + w * 256), "v"(65536 + tid * 128) : "memory");
    } else {
      int z_ = q - K2_NWIN - K2_NWOUT;
      u32x4* st = (u32x4*)(ws + WS_STAT) + z_ * 2048 + tid;
      u32 fv = z_ < 8 ? ~0u : 0u;
      u32x4 f = {fv, fv, fv, fv};
      st[0] = f; st[512] = f;
      if (z_ < 8) { st[1024] = f; st[1536] = f; }
    }
    STAMP_OUT(1);
    return;
  }
  const int kt = blockIdx.x;
  const int bp = tid & 255, e = w >> 2;
  const u32* t1 = (const u32*)(ws + WS_T1) + (size_t)kt * 2048 + bp;
  u32 uv[8];
#pragma unroll
  for (int a = 0; a < 8; ++a) uv[a] = t1[a * 256];
  const u32x4 wm4 = ((const u32x4*)(ws + WS_WMAX))[lane];
  glds(ws + WS_WT + (size_t)kt * 8192, ((tid & 255) * 2 + (tid >> 8)) * 16, lds_addr(sm) + K2_W + w * 1024);
  const float rs8 = 0.35355339059327376f;
  f4 tax, tay, t1x, t1y, t3x, t3y;
  u32x4 Fr, Fi, Gr, Gi;
#pragma unroll
  for (int i = 0; i < 4; ++i) {
    float a0 = (float)(bp * (2 * i + e)) * (1.0f / 2048.0f);
    float a1 = (float)((4 * g + i) * n) * (1.0f / 256.0f);
    float a3 = (float)((16 * (4 * g + i) + n) * w) * (1.0f / 2048.0f);
    float a4 = (float)(((4 * g + i) * n) & 15) * (1.0f / 16.0f);
    tax[i] = __builtin_amdgcn_cosf(a0); tay[i] = __builtin_amdgcn_sinf(a0);
    t1x[i] = __builtin_amdgcn_cosf(a1); t1y[i] = __builtin_amdgcn_sinf(a1);
    t3x[i] = __builtin_amdgcn_cosf(a3); t3y[i] = __builtin_amdgcn_sinf(a3);
    float c = __builtin_amdgcn_cosf(a4), sn_ = __builtin_amdgcn_sinf(a4);
    Fr[i] = pk_h(c, sn_); Fi[i] = pk_h(-sn_, c);
    Gr[i] = pk_h(c, -sn_); Gi[i] = pk_h(sn_, c);
  }
  tax *= rs8; tay *= -rs8; t1x *= 0.25f; t1y *= 0.25f; t3x *= 0.25f * rs8; t3y *= 0.25f * rs8;
  STAMP(1);
  SB();
  {
    f4 ar, ai, br, bi, xr, xi;
#pragma unroll
    for (int a = 0; a < 4; ++a) { ar[a] = h_lo(uv[a]); ai[a] = h_hi(uv[a]); br[a] = h_lo(uv[a + 4]); bi[a] = h_hi(uv[a + 4]); }
    radix8_half<-1>(e, ar, ai, br, bi, xr, xi);
    u32x4 o = cmul_pk(xr, xi, tax, tay);
    u32* ex = (u32*)sm + (bp & 15) * 20 + (bp >> 4) + e * 320;
#pragma unroll
    for (int m = 0; m < 4; ++m) ex[m * 640] = o[m];
  }
  STAMP(3);
  int wex = (int)(wave_umax(umax(umax(wm4[0], wm4[1]), umax(wm4[2], wm4[3]))) >> 7);
  wex = wex < 4 ? 4 : (wex > 254 ? 254 : wex);
  const float wk = __builtin_bit_cast(float, (u32)(256 - wex) << 23) * 0.25f;
  if (kt == 0 && tid == 0) ((u32*)(ws + WS_WMAX))[256] = (u32)(wex - 2) << 23;
  wait_vm<0>();
  __syncthreads();
  STAMP(4);
  const f4 z = {0.f, 0.f, 0.f, 0.f};
  u32x4 fa = *(const u32x4*)(sm + (w * 320 + n * 20 + 4 * g) * 4);
  f4 wx, wy;
#pragma unroll
  for (int r = 0; r < 4; ++r) {
    u32 v = *(const u32*)(sm + K2_W + ((w >> 2) * 256 + n + 16 * (4 * g + r)) * 16 + (w & 3) * 4);
    wx[r] = bf_lo(v); wy[r] = bf_hi(v);
  }
  wx *= wk; wy *= wk;
  f4 re = mfma_h(fa, Fr, z), im = mfma_h(fa, Fi, z);
  fa = cmul_pk(re, im, t1x, -t1y);
  re = mfma_h(Fr, fa, z); im = mfma_h(Fi, fa, z);
  fa = cmul_pk(re, im, wx, wy);
  re = mfma_h(fa, Gr, z); im = mfma_h(fa, Gi, z);
  fa = cmul_pk(re, im, t1x, t1y);
  re = mfma_h(Gr, fa, z); im = mfma_h(Gi, fa, z);
  {
    float* ex = (float*)(sm + 10240) + w * 256 + 64 * g + n;
    f4 a = re * t3x - im * t3y, b = re * t3y + im * t3x;
#pragma unroll
    for (int r = 0; r < 4; ++r) { ex[16 * r] = a[r]; ex[2048 + 16 * r] = b[r]; }
  }
  STAMP(5);
  __syncthreads();
  STAMP(6);
  {
    const float* ex = (const float*)(sm + 10240) + bp;
    f4 ar, ai, br, bi, xr, xi;
#pragma unroll
    for (int j = 0; j < 4; ++j) {
      ar[j] = ex[j * 256]; br[j] = ex[(j + 4) * 256];
      ai[j] = ex[2048 + j * 256]; bi[j] = ex[2048 + (j + 4) * 256];
    }
    radix8_half<1>(e, ar, ai, br, bi, xr, xi);
    u32* t2p = (u32*)(ws + WS_T2) + (size_t)kt * 2048 + bp + e * 256;
#pragma unroll
    for (int m = 0; m < 4; ++m) t2p[m * 512] = pk_h(xr[m], xi[m]);
  }
  STAMP(7);
  STAMP_OUT(1);
}

constexpr int SLOT = 16384, NU = 8;
constexpr int RED = NU * SLOT, MK_LDS = RED + 32768;
constexpr int P3_TAB = 98304, P3_Z = P3_TAB + 12288, P3_ZSTR = 420, P3_OUT = P3_TAB + 26624;
constexpr int G3_RED = 138240;
constexpr int SPIN_MAX = 1 << 16;

DI void st16_dev(__amdgpu_buffer_rsrc_t r, u32 off, u32x4 v) { __builtin_amdgcn_raw_buffer_store_b128(v, r, off, 0, 16); }
DI void st8_dev(__amdgpu_buffer_rsrc_t r, u32 off, u32x2 v) { __builtin_amdgcn_raw_buffer_store_b64(v, r, off, 0, 16); }
DI u32x4 ld16_dev(__amdgpu_buffer_rsrc_t r, u32 off) { return __builtin_amdgcn_raw_buffer_load_b128(r, off, 0, 16); }
DI u32x2 ld8_dev(__amdgpu_buffer_rsrc_t r, u32 off) { return __builtin_amdgcn_raw_buffer_load_b64(r, off, 0, 16); }
DI void set_flag(char* ws, int off) {
  __hip_atomic_store((GLB_AS u32*)(ws + WS_FLAG + off), 1u, __ATOMIC_RELAXED, __HIP_MEMORY_SCOPE_AGENT);
}
DI void poll_flags(char* ws, int off, int mask, int lane) {
  const GLB_AS u32* p = (const GLB_AS u32*)(ws + WS_FLAG + off) + (lane & mask);
  for (int spin = 0; spin < SPIN_MAX; ++spin) {
    u32 v = __hip_atomic_load(p, __ATOMIC_RELAXED, __HIP_MEMORY_SCOPE_AGENT);
    if (__all(v != 0u)) break;
  }
  asm volatile("" ::: "memory");
}

__global__ __launch_bounds__(512, 2) void k3_mega(const char* __restrict__ x, const char* __restrict__ wsp,
                                                  const char* __restrict__ bsp, char* __restrict__ ws,
                                                  u32x2* __restrict__ out) {
  extern __shared__ __attribute__((aligned(16))) char sm[];
  const int tid = threadIdx.x, lane = tid & 63, w = uni(tid >> 6);
  const int n = lane & 15, g = lane >> 4;
  STAMP_DECL; STAMP(0);
  const int xc = blockIdx.x & 7, bi = blockIdx.x >> 3;
  const int rt = (xc >> 1) * 8 + (bi >> 2), cg = (xc & 1) * 4 + (bi & 3);
  const int nt_all = (cg & 3) == 3 ? 6 : 7, t0 = cg * 7 - (cg >> 2);
  const int mt = w & 3, h = w >> 2;
  const int ncnt = h ? nt_all - 4 : 4, nfirst = 4 * h;
  const int srow = rt * 64 + mt * 16;
  const u32 smb = lds_addr(sm);
  const __amdgpu_buffer_rsrc_t wsr = __builtin_amdgcn_make_buffer_rsrc(ws, 0, (int)WS_END, 0x00020000);
  const f4 z = {0.f, 0.f, 0.f, 0.f};
  const u32 l16 = lane * 16;

  const u32x4 srd = {(u32)(size_t)ws, (u32)((size_t)ws >> 32) & 0xffffu, (u32)WS_END, 0x00020000u};
  const u32 l16b = (lane < nt_all * 16 - 64 ? lane : nt_all * 16 - 65) * 16;
  const u32 ldw = smb + w * 2048;
  const u32 winb = (u32)WS_WIN + ((8 * h + mt) * F + t0 * 16) * 16;
  auto g1_issue = [&](auto U, int q) {
    constexpr int u = U.value, v = u < 10 ? u : u - 10;
    const u32 so = winb + ((v >> 1) * 16 + (v & 1) * 4) * (F * 16) + (u < 10 ? FH * 16 : 0);
    if (q == 0) bdma0<(u % NU) * SLOT, false>(srd, l16, so, ldw);
    else bdma1<false>(srd, l16b, so);
  };
  const u32 zb = (u32)WS_Z2 + ((8 * h + mt) * FH + t0 * 16) * 16;
  auto g2_issue = [&](auto U, int q) {
    constexpr int u = U.value;
    const u32 so = zb + ((u >> 1) * 16 + (u & 1) * 4) * (FH * 16);
    if (q == 0) bdma0<(u % NU) * SLOT, true>(srd, l16, so, ldw);
    else bdma1<true>(srd, l16b, so);
  };
  const u32 wob = (u32)WS_WOUT + cg * 138240 + w * 2048;
  auto wo_issue = [&](auto V, int q) {
    if (q == 0) bdma0<V.value * SLOT, false>(srd, l16, wob + V.value * SLOT, ldw);
    else bdma1<false>(srd, l16, wob + V.value * SLOT);
  };
  u32 fbA0 = smb + (4 * h + g) * 2048 + n * 16 + h * 1024, fbB0 = fbA0 - h * 1792;
  u32 fbA1 = fbA0 + 65536, fbB1 = fbB0 + 65536;
  asm("" : "+v"(fbA1), "+v"(fbB0), "+v"(fbB1));
  auto frag = [&](auto U, int j) {
    constexpr int sl = U.value % NU;
    return lds_rd((j < 3 ? (sl < 4 ? fbA0 : fbA1) : (sl < 4 ? fbB0 : fbB1)) + (sl & 3) * SLOT + j * 256);
  };
  const u32 hoff = ((u32)(rt * 8 + 2 * mt + (n >> 3)) * 80) * 128 + (n & 7) * 16;
  float biasv;
  u32x4 wf[32];
  const u32 wvo = (lane >> 3) * 4096 + (((lane & 7) ^ (lane >> 4)) << 4);
  const char* wsb = wsp + (size_t)srow * 4096 + h * 128;
  auto wsp_issue = [&](int i, int half) {
    const int sl = i < 8 ? (i & 3) : (i + 4) % NU;
    glds(wsb + i * 256 + half * 32768, wvo ^ (half << 6), ldw + sl * SLOT + half * 1024);
  };
  u32 wr0 = ldw + n * 128 + ((g ^ (n >> 1)) << 4), wr1 = wr0 + 65536;
  asm("" : "+v"(wr1));
  auto wsp_read = [&](int i) {
    const int sl = i < 8 ? (i & 3) : (i + 4) % NU;
    const u32 a = (sl < 4 ? wr0 : wr1);
    wf[2 * i] = lds_rd(a + (sl & 3) * SLOT);
    wf[2 * i + 1] = lds_rd((a ^ 64) + (sl & 3) * SLOT);
  };
#define R2(k) do { SB(); wsp_issue(4 + ((k) >> 1), (k) & 1); SB(); } while (0)

  {
    const int rg = rt * 8 + cg, s = rg * 8 + w;
    u32x4 z0, z1;
    u32x2 xv[3];
    u32 braw;
    {
      int tc = tid < 336 ? tid : 335;
      int k1 = tc >> 4, k2 = tc & 15, k = k1 + 40 * k2;
      int kt = k2 < 8 ? k : 640 - k;
      const char* t2b = ws + WS_T2 + rg * 32;
      const char* xb = x + (size_t)s * 1280;
      asm volatile("global_load_dwordx4 %0, %2, %3\n\tglobal_load_dwordx4 %1, %2, %3 offset:16"
                   : "=&v"(z0), "=&v"(z1) : "v"(kt * 8192), "s"(t2b) : "memory");
      asm volatile("global_load_dwordx2 %0, %3, %5\n\tglobal_load_dwordx2 %1, %3, %5 offset:512\n\tglobal_load_dwordx2 %2, %4, %5 offset:1024"
                   : "=&v"(xv[0]), "=&v"(xv[1]), "=&v"(xv[2]) : "v"(n * 32 + g * 8), "v"((n & 7) * 32 + g * 8), "s"(xb) : "memory");
      asm volatile("global_load_ushort %0, %1, %2" : "=&v"(braw) : "v"((srow + n) * 2), "s"(bsp) : "memory");
    }
    glds(&g_tab_inv.v[w][0][0], l16, smb + P3_TAB + w * 1024);
    glds(&g_tab_inv.v[8 + (w & 3)][0][0], l16, smb + P3_TAB + (8 + (w & 3)) * 1024);
    const float winv = ((const float*)(ws + WS_WMAX))[256];
#pragma unroll
    for (int q = 0; q < 8; ++q) wsp_issue(q >> 1, q & 1);
    STAMP(1);
    asm volatile("s_waitcnt vmcnt(8)" : "+v"(z0), "+v"(z1), "+v"(xv[0]), "+v"(xv[1]), "+v"(xv[2]), "+v"(braw)::"memory");
    STAMP(2);
    biasv = bf_lo(braw);
    if (tid < 336) {
      u32* zp = (u32*)(sm + P3_Z) + (tid >> 4) * 20 + (tid & 15);
#pragma unroll
      for (int i = 0; i < 4; ++i) { zp[i * P3_ZSTR] = z0[i]; zp[(4 + i) * P3_ZSTR] = z1[i]; }
    }
    lgkm0(); bar();
    STAMP(3);
    const u32x4* tab = (const u32x4*)(sm + P3_TAB) + lane;
    u32x4 gr = tab[0], gi = tab[64];
    u32x4 fv[2];
#pragma unroll
    for (int m = 0; m < 2; ++m) {
      int k1 = 16 * m + n;
      k1 = k1 <= 20 ? k1 : 20;
      u32x4 fa = *(const u32x4*)(sm + P3_Z + (w * P3_ZSTR + k1 * 20 + 4 * g) * 4);
      f4 re = mfma_h(fa, gr, z), im = mfma_h(fa, gi, z);
      fv[m] = cmul_pk(re, im, __builtin_bit_cast(f4, tab[(2 + 2 * m) * 64]), __builtin_bit_cast(f4, tab[(3 + 2 * m) * 64]));
    }
    u32x4 tb[6];
#pragma unroll
    for (int i = 0; i < 6; ++i) tb[i] = tab[(6 + i) * 64];
    wait_vm<0>();
#pragma unroll
    for (int i = 0; i < 4; ++i) wsp_read(i);
    u32x2 hp[3];
    f4 yy[3];
    const bool v2 = n < 8;
#pragma unroll
    for (int nt = 0; nt < 3; ++nt) {
      yy[nt] = mfma_h(fv[0], tb[2 * nt], z);
      yy[nt] = mfma_h(fv[1], tb[2 * nt + 1], yy[nt]);
    }
    lgkm0();
    R2(0);
#pragma unroll
    for (int nt = 0; nt < 3; ++nt) {
      f4 y = yy[nt] * winv;
      u32 y0 = pk_bf(y[0], y[1]), y1 = pk_bf(y[2], y[3]);
      hp[nt][0] = pk_bf(bf_lo(xv[nt][0]) + bf_lo(y0), bf_hi(xv[nt][0]) + bf_hi(y0));
      hp[nt][1] = pk_bf(bf_lo(xv[nt][1]) + bf_lo(y1), bf_hi(xv[nt][1]) + bf_hi(y1));
      R2(1 + nt);
    }
    const float pilot = bf_lo((u32)uni((int)hp[0][0]));
    f2 s2 = {0.f, 0.f}, q2 = {0.f, 0.f};
#pragma unroll
    for (int i = 0; i < 6; ++i) {
      u32 v = hp[i >> 1][i & 1];
      f2 d = f2{bf_lo(v), bf_hi(v)} - pilot;
      if (i >= 4) d = v2 ? d : f2{0.f, 0.f};
      s2 += d;
      q2 += d * d;
    }
    R2(4);
    char* ob = sm + P3_OUT + ((w ^ (n & 7)) << 4) + (g >> 1) * 128 + n * 256;
    *(u32x4*)(ob + 10240 + (g & 1) * 4096) = widen(hp[0], hp[1]);
    R2(5);
    const float md = wave_sum(opq(s2.x) + opq(s2.y)) * (1.0f / 640.0f);
    const float qd = wave_sum(opq(q2.x) + opq(q2.y)) * (1.0f / 640.0f);
    R2(6);
    const f2 ln = ln_scalars(pilot + md, __builtin_fmaxf(qd - md * md, 0.f));
    R2(7);
    u32x2 on[3];
#pragma unroll
    for (int nt = 0; nt < 3; ++nt) {
      on[nt] = u32x2{ln_pair(hp[nt][0], ln.x, ln.y, ln.x, ln.y), ln_pair(hp[nt][1], ln.x, ln.y, ln.x, ln.y)};
    }
    *(u32x4*)(ob + (g & 1) * 4096) = widen(on[0], on[1]);
    u32x4 w2 = widen(on[2], hp[2]);
    if (v2) *(u32x4*)(ob + 8192 + (g & 1) * 10240) = w2;
    STAMP(4);
    lgkm0(); bar();
    const u32 go = (u32)rg * 10240 + (tid >> 3) * 128 + (((tid ^ (tid >> 4)) & 7) << 4);
    const char* rp = sm + P3_OUT + tid * 16;
    u32x4 c0 = *(const u32x4*)(rp), c2 = *(const u32x4*)(rp + 10240);
    u32x4 c1 = *(const u32x4*)(rp + (tid < 128 ? 8192 : (tid < 256 ? 8192 + 8192 : 0)));
    st16_dev(wsr, (u32)WS_HN + go, c0);
    st16_dev(wsr, tid < 128 ? (u32)WS_HN + go + 8192 : (tid < 256 ? (u32)WS_H + go + 8192 - 2048 : 0x7ffffff0u), c1);
    wait_vm<2>();
#pragma unroll
    for (int i = 4; i < 8; ++i) wsp_read(i);
    wait_vm<0>();
    lgkm0(); bar();
    STAMP(5);
    if (tid == 0) set_flag(ws, rt * 128 + cg * 4);
    st16_dev(wsr, (u32)WS_H + go, c2);
    static_for<0, 6>([&](auto U) { g1_issue(U, 0); g1_issue(U, 1); });
    if (w == 0) poll_flags(ws, rt * 128, 7, lane);
    wait_vm<8>();
    bar();
    STAMP(6);
  }

  u32x2 z1p[4];
  f4 acc[7], fin[4];
  u32x4 fa[7];
  auto reduce_put = [&]() {
    f4* red = (f4*)(sm + RED) + w * 256 + lane;
#pragma unroll
    for (int j = 0; j < 4; ++j) red[j * 64] = acc[3 + j];
  };
  auto reduce_get = [&]() {
    const f4* red = (const f4*)(sm + RED) + (w ^ 4) * 256 + h * 64 + lane;
#pragma unroll
    for (int j = 0; j < 4; ++j) fin[j] = acc[j] + red[j * 64];
  };
  {
    u32x4 af[10];
    {
      const u32 ao = (u32)WS_HN + hoff + (8 * h + g) * 128;
      static_for<0, 10>([&](auto U) {
        constexpr int u = U.value;
        ld16_async<((u >> 1) & 1) * 2048 + (u & 1) * 512>(af[u], srd, ao, (u >> 2) * 4096);
      });
    }
#pragma unroll
    for (int j = 0; j < 7; ++j) fa[j] = frag(IC<0>{}, j);
    STAMP(7);
    u32x2 z2p[4];
    u32x2 gr[4];
    const u32 stoff = (u32)WS_STAT + ((u32)(srow + 4 * g) * 16 + n) * 8;
    static_for<0, 20>([&](auto U) {
      constexpr int u = U.value;
      if constexpr (u < 10) {
        wait_vm_tie<((u & 1) && u >= 5) ? 6 : 9 + u>(af[u]);
      } else if constexpr (u == 15) {
        asm volatile("s_waitcnt vmcnt(2)" : "+v"(gr[0]), "+v"(gr[1]), "+v"(gr[2]), "+v"(gr[3])::"memory");
      } else if constexpr (u == 11 || u == 13 || u == 19) {
        wait_vm<6>();
      }
      if constexpr (u & 1) bar();
      if constexpr (u == 19) {
        if (tid == 0) set_flag(ws, 4096 + cg * 128 + rt * 4);
        STAMP(11);
      }
      constexpr bool has_next = u + 1 < 20;
#pragma unroll
      for (int j = 0; j < 7; ++j) {
        if (j == 0) wait_lgkm<3>();
        if (j == 4) wait_lgkm<has_next ? 4 : 0>();
        if constexpr (u < 10) acc[j] = mfma_b(af[u], fa[j], u == 0 ? z : acc[j]);
        else acc[j] = mfma_b(fa[j], af[u - 10], u == 10 ? z : acc[j]);
        SB();
        if constexpr (has_next) fa[j] = frag(IC<u + 1>{}, j);
        if (j == 1 || j == 4) {
          if constexpr (u + 6 < 20) g1_issue(IC<u + 6>{}, j == 4);
          else wsp_issue(u - 6, j == 4);
        }
        if constexpr (u == 13) {
          if (j >= 5) {
            asm volatile("global_load_dwordx2 %0, %1, %2 offset:%3 sc1" : "=v"(gr[2 * j - 10]) : "v"(stoff), "s"(ws), "n"((2 * j - 10) * 128) : "memory");
            asm volatile("global_load_dwordx2 %0, %1, %2 offset:%3 sc1" : "=v"(gr[2 * j - 9]) : "v"(stoff), "s"(ws), "n"((2 * j - 9) * 128) : "memory");
          }
        }
        SB();
      }
      if constexpr (u == 9) {
        STAMP(8);
        reduce_put();
        lgkm0(); bar();
        reduce_get();
        f4 sum = z, sq = z;
#pragma unroll
        for (int j = 0; j < 4; ++j) {
          if (j < 2 || j < ncnt) {
            f2 a = gelu2f(pk_bf(fin[j][0], fin[j][1])), b = gelu2f(pk_bf(fin[j][2], fin[j][3]));
            z2p[j] = u32x2{pk_bf(a.x, a.y), pk_bf(b.x, b.y)};
            f4 zv = {a.x, a.y, b.x, b.y};
            sum += zv;
            sq += zv * zv;
          } else {
            z2p[j] = u32x2{0u, 0u};
          }
        }
        const f2 rs = row16_reduce_scatter(sum, sq);
        u32x2 gv = {as_u(rs.x), as_u(rs.y) & 0x7fffffffu};
        st8_dev(wsr, (n & 3) ? 0x7ffffff0u : (u32)WS_STAT + ((u32)(srow + (lane >> 2)) * 16 + cg * 2 + h) * 8, gv);
        STAMP(9);
        SB();
      }
      if constexpr (u == 15) {
        bool ok = !((gr[0][1] | gr[1][1] | gr[2][1] | gr[3][1]) >> 31);
        for (int spin = 0; !__all(ok) && spin < SPIN_MAX; ++spin) {
          asm volatile("" ::: "memory");
#pragma unroll
          for (int r = 0; r < 4; ++r) gr[r] = ld8_dev(wsr, stoff + r * 128);
          ok = !((gr[0][1] | gr[1][1] | gr[2][1] | gr[3][1]) >> 31);
        }
        const f2 rs = row16_reduce_scatter(f4{as_f(gr[0][0]), as_f(gr[1][0]), as_f(gr[2][0]), as_f(gr[3][0])},
                                           f4{as_f(gr[0][1]), as_f(gr[1][1]), as_f(gr[2][1]), as_f(gr[3][1])});
        const float mean = rs.x * (1.0f / 864.0f);
        const f2 lq = ln_scalars(mean, __builtin_fmaxf(rs.y * (1.0f / 864.0f) - mean * mean, 0.f));
        const float m0 = dpp<0x150>(lq.x), m1 = dpp<0x154>(lq.x), m2 = dpp<0x158>(lq.x), m3 = dpp<0x15c>(lq.x);
        const float r0 = dpp<0x150>(lq.y), r1 = dpp<0x154>(lq.y), r2 = dpp<0x158>(lq.y), r3 = dpp<0x15c>(lq.y);
        u32x2 o[4];
#pragma unroll
        for (int j = 0; j < 4; ++j) o[j] = u32x2{ln_pair(z2p[j][0], m0, r0, m1, r1), ln_pair(z2p[j][1], m2, r2, m3, r3)};
        const u32 zo = (u32)WS_Z2 + ((u32)((srow >> 3) + (g >> 1)) * FH + (t0 + nfirst + (g & 1)) * 16 + n) * 16;
        st16_dev(wsr, zo, widen(o[0], o[1]));
        u32x4 w2 = widen(o[2], o[3]);
        st16_dev(wsr, 2 + (g & 1) < ncnt ? zo + 512 : 0x7ffffff0u, w2);
        STAMP(10);
        SB();
      }
    });
    reduce_put();
    lgkm0(); bar();
    STAMP(12);
#pragma unroll
    for (int q = 0; q < 4; ++q) wsp_issue(14 + (q >> 1), q & 1);
    reduce_get();
#pragma unroll
    for (int j = 0; j < 4; ++j) {
      z1p[j] = u32x2{0u, 0u};
      if (j < 2 || j < ncnt) z1p[j] = gelu4(fin[j]);
      asm volatile("" : "+v"(z1p[j]));
    }
    SB();
    if (w == 0) poll_flags(ws, 4096 + cg * 128, 31, lane);
    wait_vm<0>();
#pragma unroll
    for (int i = 8; i < 16; ++i) wsp_read(i);
    lgkm0(); bar();
    STAMP(13);
  }

  {
    static_for<0, 6>([&](auto U) { g2_issue(U, 0); g2_issue(U, 1); });
    wait_vm<8>();
    bar();
    STAMP(14);
#pragma unroll
    for (int j = 0; j < 7; ++j) fa[j] = frag(IC<0>{}, j);
    static_for<0, 32>([&](auto U) {
      constexpr int u = U.value;
      if constexpr (u & 1) {
        wait_vm<6>();
        bar();
      }
#pragma unroll
      for (int j = 0; j < 7; ++j) {
        if (j == 0) wait_lgkm<3>();
        if (j == 4) wait_lgkm<(u + 1 < 32) ? 4 : 0>();
        acc[j] = mfma_b(fa[j], wf[u], u == 0 ? z : acc[j]);
        SB();
        if constexpr (u + 1 < 32) fa[j] = frag(IC<u + 1>{}, j);
        if (j == 1 || j == 4) {
          if constexpr (u + 6 < 32) g2_issue(IC<u + 6>{}, j == 4);
          else wo_issue(IC<u + 6 - 32>{}, j == 4);
        }
        SB();
      }
    });
    STAMP(15);
    reduce_put();
    lgkm0(); bar();
    wo_issue(IC<6>{}, 0); wo_issue(IC<6>{}, 1); wo_issue(IC<7>{}, 0); wo_issue(IC<7>{}, 1);
    reduce_get();
    u32x2 o[4];
#pragma unroll
    for (int j = 0; j < 4; ++j) {
      u32 g0 = pk_bf(fin[j][0], fin[j][1]), g1 = pk_bf(fin[j][2], fin[j][3]);
      g0 = pk_bf(bf_lo(g0) + biasv, bf_hi(g0) + biasv);
      g1 = pk_bf(bf_lo(g1) + biasv, bf_hi(g1) + biasv);
      o[j] = u32x2{pk_bf(bf_lo(g0) * bf_lo(z1p[j][0]), bf_hi(g0) * bf_hi(z1p[j][0])),
                   pk_bf(bf_lo(g1) * bf_lo(z1p[j][1]), bf_hi(g1) * bf_hi(z1p[j][1]))};
    }
    const u32 po = (u32)WS_P + ((u32)(rt * 108 + 2 * (t0 + nfirst + (g & 1)) + (g >> 1)) * 64 + mt * 16 + n) * 16;
    st16_dev(wsr, po, widen(o[0], o[1]));
    u32x4 w2 = widen(o[2], o[3]);
    st16_dev(wsr, 2 + (g & 1) < ncnt ? po + 4096 : 0x7ffffff0u, w2);
    STAMP(16);
  }

  {
    wait_vm<0>();
    lgkm0(); bar();
    STAMP(17);
    if (tid == 0) set_flag(ws, 8192 + rt * 128 + cg * 4);
    if (w == 0) poll_flags(ws, 8192 + rt * 128, 7, lane);
    else glds(ws + WS_WOUT + (size_t)cg * 138240 + (127 + w) * 1024, l16, smb + (127 + w) * 1024);
    bar();
    STAMP(18);
    u32x4 pf[14];
#pragma unroll
    for (int i = 0; i < 14; ++i) {
      int ks = h * 13 + i;
      pf[i] = ld16_dev(wsr, (u32)WS_P + ((u32)(rt * 108 + 4 * ks + g) * 64 + mt * 16 + n) * 16);
    }
    const int dfirst = h ? 3 : 0, dcnt = h ? 2 : 3;
    u32x2 hv[3];
#pragma unroll
    for (int j = 0; j < 3; ++j) {
      int jj = dfirst + (j < dcnt ? j : 0);
      hv[j] = ld8_dev(wsr, (u32)WS_H + hoff + (u32)(cg * 10 + 2 * jj + (g >> 1)) * 128 + (g & 1) * 8);
    }
    wait_vm<17>();
    bar();
    STAMP(19);
    if (h) pf[0] = u32x4{0u, 0u, 0u, 0u};
    u32 wp0 = smb + ((h * 13 * 4 + g) * 80 + n) * 16, wp1 = wp0 + 7 * 5120;
    asm("" : "+v"(wp1));
#pragma unroll
    for (int j = 0; j < 5; ++j) fa[j] = lds_rd(wp0 + j * 256);
    static_for<0, 14>([&](auto I) {
      constexpr int i = I.value;
#pragma unroll
      for (int j = 0; j < 5; ++j) {
        if (j == 0) wait_lgkm<2>();
        if (j == 3) wait_lgkm<(i + 1 < 14) ? 3 : 0>();
        acc[j] = mfma_b(fa[j], pf[i], i == 0 ? z : acc[j]);
        SB();
        if constexpr (i + 1 < 14) fa[j] = lds_rd((i + 1 < 7 ? wp0 + (i + 1) * 5120 : wp1 + (i - 6) * 5120) + j * 256);
        SB();
      }
    });
    STAMP(20);
    f4* red = (f4*)(sm + G3_RED) + mt * 5 * 64 + lane;
    if (h == 0) {
#pragma unroll
      for (int j = 3; j < 5; ++j) red[j * 64] = acc[j];
    } else {
#pragma unroll
      for (int j = 0; j < 3; ++j) red[j * 64] = acc[j];
    }
    lgkm0(); bar();
    if (h == 0) {
#pragma unroll
      for (int j = 0; j < 3; ++j) fin[j] = acc[j] + red[j * 64];
    } else {
#pragma unroll
      for (int j = 0; j < 2; ++j) fin[j] = acc[3 + j] + red[(3 + j) * 64];
      fin[2] = z;
    }
#pragma unroll
    for (int j = 0; j < 3; ++j) {
      if (j < dcnt) {
        u32 o0 = pk_bf(fin[j][0], fin[j][1]), o1 = pk_bf(fin[j][2], fin[j][3]);
        u32x2 o = {pk_bf(bf_lo(hv[j][0]) + bf_lo(o0), bf_hi(hv[j][0]) + bf_hi(o0)),
                   pk_bf(bf_lo(hv[j][1]) + bf_lo(o1), bf_hi(hv[j][1]) + bf_hi(o1))};
        out[(size_t)(srow + n) * 160 + (cg * 5 + dfirst + j) * 4 + g] = o;
      }
    }
    STAMP(21);
    STAMP_OUT(2);
  }
}

extern "C" void kernel_launch(void* const* d_in, const int* in_sizes, int n_in, void* d_out, int out_size,
                              void* d_ws, size_t ws_size, hipStream_t stream) {
  (void)in_sizes; (void)n_in; (void)out_size; (void)ws_size;
  char* ws = (char*)d_ws;
  (void)hipFuncSetAttribute((const void*)k3_mega, hipFuncAttributeMaxDynamicSharedMemorySize, MK_LDS);
  k1_fwd<<<512, 512, K1_LDS, stream>>>((const u32*)d_in[0], (const u32x4*)d_in[1], (const u32x4*)d_in[2], ws);
  k2_col<<<KT + K2_NWIN + K2_NWOUT + K2_NSTAT + K2_NPF, 512, K2_LDS, stream>>>(
      (const u32x2*)d_in[4], (const u32x2*)d_in[7], (const char*)d_in[5], ws);
  k3_mega<<<256, 512, MK_LDS, stream>>>((const char*)d_in[0], (const char*)d_in[5], (const char*)d_in[6], ws,
                                        (u32x2*)d_out);
}
```

```cpp
#include <hip/hip_runtime.h>
#include <stdint.h>
#include <type_traits>

typedef unsigned int u32;
typedef unsigned short u16;
typedef unsigned long long u64;
typedef _Float16 h2 __attribute__((ext_vector_type(2)));
typedef _Float16 h8 __attribute__((ext_vector_type(8)));
typedef short s4 __attribute__((ext_vector_type(4)));
typedef __bf16 b2 __attribute__((ext_vector_type(2)));
typedef __bf16 b8 __attribute__((ext_vector_type(8)));
typedef float f2 __attribute__((ext_vector_type(2)));
typedef float f4 __attribute__((ext_vector_type(4)));
typedef u32 u32x2 __attribute__((ext_vector_type(2)));
typedef u32 u32x4 __attribute__((ext_vector_type(4)));
#define DI __device__ __forceinline__
#define LDS_AS __attribute__((address_space(3)))
#define GLB_AS __attribute__((address_space(1)))

constexpr int S = 2048, D = 640, FH = 864, F = 1728, KT = 321;
constexpr int TS = 2048 + 32;

constexpr size_t MB = 1u << 20, QMB = MB / 4;
constexpr size_t SP_T = 0;
constexpr size_t SP_WT = 3 * MB;
constexpr size_t SP_WMAX = 6 * MB;
constexpr size_t WS_H = 0;
constexpr size_t WS_HN = 10 * QMB;
constexpr size_t WS_WIN = 20 * QMB;
constexpr size_t WS_WOUT = 29 * QMB;
constexpr size_t WS_Z2 = 34 * QMB;
constexpr size_t WS_P = 48 * QMB;
constexpr size_t WS_STAT = 62 * QMB;
constexpr size_t WS_FLAG = WS_STAT + 262144;
constexpr size_t WS_END = 16 * MB;

constexpr double kPi = 3.14159265358979323846264338327950288;
constexpr double c_sin_small(double x) {
  double t = x, s = x;
  for (int i = 1; i < 10; ++i) { t *= -x * x / ((2 * i) * (2 * i + 1)); s += t; }
  return s;
}
constexpr double c_cos_small(double x) {
  double t = 1, s = 1;
  for (int i = 1; i < 10; ++i) { t *= -x * x / ((2 * i - 1) * (2 * i)); s += t; }
  return s;
}
struct Cos640 { double c[640]; };
constexpr Cos640 make_cos640() {
  Cos640 t{};
  for (int j = 0; j <= 160; ++j) {
    double v = (j <= 80) ? c_cos_small(2 * kPi * j / 640) : c_sin_small(2 * kPi * (160 - j) / 640);
    if (j == 160) v = 0.0;
    t.c[j] = v;
    t.c[(640 - j) % 640] = v;
    t.c[320 - j] = -v;
    t.c[(320 + j) % 640] = -v;
  }
  return t;
}
constexpr Cos640 kCos = make_cos640();
constexpr double cs(int j) { return kCos.c[((j % 640) + 640) % 640]; }
constexpr double sn(int j) { return kCos.c[(((j - 160) % 640) + 640) % 640]; }
constexpr u32 pkh(double lo, double hi) {
  return (u32)__builtin_bit_cast(u16, (_Float16)(float)lo) | ((u32)__builtin_bit_cast(u16, (_Float16)(float)hi) << 16);
}
constexpr u32 fb(double v) { return __builtin_bit_cast(u32, (float)v); }
constexpr double kRs40 = 0.15811388300841896659994467722163593;

struct Tab { u32 v[12][64][4]; };
constexpr Tab make_tab_fwd() {
  Tab t{};
  for (int l = 0; l < 64; ++l) {
    int n = l & 15, g = l >> 4;
    for (int nt = 0; nt < 2; ++nt) {
      int k1 = 16 * nt + n;
      bool ok = k1 <= 20;
      for (int q = 0; q < 4; ++q) {
        int a0 = (q < 2) ? 4 * g + 2 * q : 16 + 4 * g + 2 * (q - 2), a1 = a0 + 1;
        t.v[0 + nt][l][q] = ok ? pkh(cs(16 * a0 * k1), cs(16 * a1 * k1)) : 0u;
        t.v[2 + nt][l][q] = ok ? pkh(-sn(16 * a0 * k1), -sn(16 * a1 * k1)) : 0u;
      }
      for (int q = 0; q < 2; ++q) {
        int a0 = 32 + 4 * g + 2 * q, a1 = a0 + 1;
        bool v = ok && a0 < 40;
        t.v[4][l][2 * nt + q] = v ? pkh(cs(16 * a0 * k1), cs(16 * a1 * k1)) : 0u;
        t.v[5][l][2 * nt + q] = v ? pkh(-sn(16 * a0 * k1), -sn(16 * a1 * k1)) : 0u;
      }
      for (int r = 0; r < 4; ++r) {
        int b = 4 * g + r;
        t.v[6 + 2 * nt][l][r] = fb(cs(b * k1) * kRs40);
        t.v[7 + 2 * nt][l][r] = fb(-sn(b * k1) * kRs40);
      }
    }
    for (int q = 0; q < 4; ++q) {
      int b = 4 * g + q, k2 = n;
      double c = cs(40 * b * k2) * 0.25, s = sn(40 * b * k2) * 0.25, sg = (k2 >= 8) ? -1.0 : 1.0;
      t.v[10][l][q] = pkh(c, s);
      t.v[11][l][q] = pkh(-s * sg, c * sg);
    }
  }
  return t;
}
constexpr Tab make_tab_inv() {
  Tab t{};
  for (int l = 0; l < 64; ++l) {
    int n = l & 15, g = l >> 4;
    for (int q = 0; q < 4; ++q) {
      int k2 = 4 * g + q, b = n;
      double c = cs(40 * b * k2) * 0.25, s = sn(40 * b * k2) * 0.25, sg = (k2 >= 8) ? -1.0 : 1.0;
      t.v[0][l][q] = pkh(c, -s * sg);
      t.v[1][l][q] = pkh(s, c * sg);
    }
    for (int mt = 0; mt < 2; ++mt) {
      for (int r = 0; r < 4; ++r) {
        int k1 = 16 * mt + 4 * g + r, b = n;
        t.v[2 + 2 * mt][l][r] = fb(cs(b * k1) * kRs40);
        t.v[3 + 2 * mt][l][r] = fb(sn(b * k1) * kRs40);
      }
      for (int nt = 0; nt < 3; ++nt) {
        int a = 16 * nt + n;
        for (int q = 0; q < 4; ++q) {
          int k1 = 16 * mt + 4 * g + q;
          double w = (k1 == 0 || k1 == 20) ? 1.0 : 2.0;
          bool ok = k1 <= 20 && a < 40;
          t.v[6 + 2 * nt + mt][l][q] = ok ? pkh(w * cs(16 * a * k1), -w * sn(16 * a * k1)) : 0u;
        }
      }
    }
  }
  return t;
}
constexpr double cs256(int j) {
  j = ((j % 256) + 256) % 256;
  if (j > 128) j = 256 - j;
  double sg = 1.0;
  if (j > 64) { j = 128 - j; sg = -1.0; }
  if (j == 64) return 0.0;
  return sg * (j <= 32 ? c_cos_small(2 * kPi * j / 256) : c_sin_small(2 * kPi * (64 - j) / 256));
}
constexpr double sn256(int j) { return cs256(j - 64); }
struct Tab2 { u32 v[8][64][4]; };
constexpr Tab2 make_tab_col() {
  Tab2 t{};
  for (int l = 0; l < 64; ++l) {
    int n = l & 15, g = l >> 4;
    int o8 = 2 * (n >> 2) + (n & 1), im = (n >> 1) & 1;
    for (int i = 0; i < 4; ++i) {
      int a = 4 * (g & 1) + i, q = 4 * g + i;
      double c8 = cs256(32 * a * o8), s8 = sn256(32 * a * o8), c16 = cs256(16 * q * n), s16 = sn256(16 * q * n);
      t.v[0][l][i] = im ? pkh(-s8, c8) : pkh(c8, s8);
      t.v[1][l][i] = im ? pkh(s8, c8) : pkh(c8, -s8);
      t.v[2][l][i] = fb(cs256(q * n));
      t.v[3][l][i] = fb(sn256(q * n));
      t.v[4][l][i] = pkh(c16, s16);
      t.v[5][l][i] = pkh(-s16, c16);
      t.v[6][l][i] = pkh(c16, -s16);
      t.v[7][l][i] = pkh(s16, c16);
    }
  }
  return t;
}
static __device__ constexpr Tab2 g_tab_col = make_tab_col();
static __device__ __attribute__((aligned(256))) char g_spec[SP_WMAX + 2048];
static __device__ constexpr Tab g_tab_fwd = make_tab_fwd();
static __device__ constexpr Tab g_tab_inv = make_tab_inv();

#define SB() __builtin_amdgcn_sched_barrier(0)
template <int V> using IC = std::integral_constant<int, V>;
template <int I, int N, class Fn> DI void static_for(Fn&& f) {
  if constexpr (I < N) { f(IC<I>{}); static_for<I + 1, N>(f); }
}
DI float as_f(u32 v) { return __builtin_bit_cast(float, v); }
DI u32 as_u(float v) { return __builtin_bit_cast(u32, v); }
DI float bf_lo(u32 v) { return __builtin_bit_cast(float, v << 16); }
DI float bf_hi(u32 v) { return __builtin_bit_cast(float, v & 0xffff0000u); }
DI u32 pk_bf(float a, float b) { f2 x = {a, b}; return __builtin_bit_cast(u32, __builtin_convertvector(x, b2)); }
DI u32 pk_h(float a, float b) { f2 x = {a, b}; return __builtin_bit_cast(u32, __builtin_convertvector(x, h2)); }
DI float rbf(float a) { return bf_lo(pk_bf(a, a)); }
DI float opq(float v) { asm("" : "+v"(v)); return v; }
template <int CTRL> DI u32 dppu(u32 v) { return (u32)__builtin_amdgcn_update_dpp(0, (int)v, CTRL, 0xf, 0xf, false); }
template <int CTRL> DI float dpp(float v) { return __builtin_bit_cast(float, dppu<CTRL>(__builtin_bit_cast(u32, v))); }
DI f2 wave_sum2(float a, float b) {
  float r, t, ra, rb;
  asm volatile("s_nop 1\n\t"
      "v_add_f32_dpp %0, %4, %4 row_ror:8 row_mask:0xf bank_mask:0x3\n\t"
      "v_add_f32_dpp %0, %5, %5 row_ror:8 row_mask:0xf bank_mask:0xc\n\t"
      "s_nop 1\n\t"
      "v_add_f32_dpp %0, %0, %0 row_half_mirror row_mask:0xf bank_mask:0xf\n\t"
      "s_nop 1\n\t"
      "v_add_f32_dpp %0, %0, %0 quad_perm:[1,0,3,2] row_mask:0xf bank_mask:0xf\n\t"
      "s_nop 1\n\t"
      "v_add_f32_dpp %1, %0, %0 quad_perm:[2,3,0,1] row_mask:0xf bank_mask:0xf\n\t"
      "v_add_f32_dpp %0, %0, %0 quad_perm:[2,3,0,1] row_mask:0xf bank_mask:0xf\n\t"
      "s_nop 1\n\t"
      "v_permlane16_swap_b32 %0, %1\n\t"
      "s_nop 1\n\t"
      "v_add_f32 %2, %0, %1\n\t"
      "v_add_f32 %0, %0, %1\n\t"
      "s_nop 1\n\t"
      "v_permlane32_swap_b32 %0, %2\n\t"
      "s_nop 1\n\t"
      "v_add_f32 %0, %0, %2\n\t"
      "s_nop 1\n\t"
      "v_mov_b32_dpp %2, %0 row_newbcast:0 row_mask:0xf bank_mask:0xf\n\t"
      "v_mov_b32_dpp %3, %0 row_newbcast:8 row_mask:0xf bank_mask:0xf"
      : "=&v"(r), "=&v"(t), "=&v"(ra), "=&v"(rb) : "v"(a), "v"(b));
  return f2{ra, rb};
}
DI u32 umax(u32 a, u32 b) { return a > b ? a : b; }
DI u32 wave_umax(u32 v) {
  v = umax(v, dppu<0x128>(v)); v = umax(v, dppu<0x124>(v)); v = umax(v, dppu<0x122>(v)); v = umax(v, dppu<0x121>(v));
  auto a = __builtin_amdgcn_permlane16_swap(v, v, false, false);
  v = umax(a[0], a[1]);
  auto b = __builtin_amdgcn_permlane32_swap(v, v, false, false);
  return umax(b[0], b[1]);
}
DI u32x4 widen(u32x2 a, u32x2 b) {
  auto x = __builtin_amdgcn_permlane16_swap(a[0], b[0], false, false);
  auto y = __builtin_amdgcn_permlane16_swap(a[1], b[1], false, false);
  return u32x4{x[0], y[0], x[1], y[1]};
}
DI u32x4 cmul_pk(f4 re, f4 im, f4 tx, f4 ty) {
  f4 a = re * tx - im * ty, b = re * ty + im * tx;
  return u32x4{pk_h(a[0], b[0]), pk_h(a[1], b[1]), pk_h(a[2], b[2]), pk_h(a[3], b[3])};
}
DI f4 mfma_h(u32x4 a, u32x4 b, f4 c) {
  return __builtin_amdgcn_mfma_f32_16x16x32_f16(__builtin_bit_cast(h8, a), __builtin_bit_cast(h8, b), c, 0, 0, 0);
}
DI f4 mfma_b(u32x4 a, u32x4 b, f4 c) {
  return __builtin_amdgcn_mfma_f32_16x16x32_bf16(__builtin_bit_cast(b8, a), __builtin_bit_cast(b8, b), c, 0, 0, 0);
}
DI u32x2 tr_read(const char* p) {
  return __builtin_bit_cast(u32x2, __builtin_amdgcn_ds_read_tr16_b64_v4i16((LDS_AS s4*)p));
}
DI void glds(const void* sbase, u32 voff, u32 lds) {
  asm volatile("s_mov_b32 m0, %2\n\ts_nop 0\n\tglobal_load_lds_dwordx4 %0, %1" ::"v"(voff), "s"(sbase), "s"(lds) : "memory");
}
template <int LOFF, bool SC1> DI void bdma0(u32x4 srd, u32 voff, u32 soff, u32 lds) {
  if (SC1) asm volatile("s_add_i32 m0, %3, %4\n\ts_nop 0\n\tbuffer_load_dwordx4 %0, %1, %2 offen sc1 lds"
                        ::"v"(voff), "s"(srd), "s"(soff), "s"(lds), "n"(LOFF) : "memory", "scc");
  else asm volatile("s_add_i32 m0, %3, %4\n\ts_nop 0\n\tbuffer_load_dwordx4 %0, %1, %2 offen lds"
                    ::"v"(voff), "s"(srd), "s"(soff), "s"(lds), "n"(LOFF) : "memory", "scc");
}
template <bool SC1> DI void bdma1(u32x4 srd, u32 voff, u32 soff) {
  if (SC1) asm volatile("buffer_load_dwordx4 %0, %1, %2 offen offset:1024 sc1 lds" ::"v"(voff), "s"(srd), "s"(soff) : "memory");
  else asm volatile("buffer_load_dwordx4 %0, %1, %2 offen offset:1024 lds" ::"v"(voff), "s"(srd), "s"(soff) : "memory");
}
template <int OFF> DI void ld16_async(u32x4& d, u32x4 srd, u32 voff, u32 soff) {
  asm volatile("buffer_load_dwordx4 %0, %1, %2, %3 offen offset:%4 sc1" : "=v"(d) : "v"(voff), "s"(srd), "s"(soff), "n"(OFF) : "memory");
}
template <int OFF> DI void ld8_async(u32x2& d, u32x4 srd, u32 voff) {
  asm volatile("buffer_load_dwordx2 %0, %1, %2, 0 offen offset:%3 sc1" : "=v"(d) : "v"(voff), "s"(srd), "n"(OFF) : "memory");
}
template <int N> DI void wait_vm_tie(u32x4& d) { asm volatile("s_waitcnt vmcnt(%1)" : "+v"(d) : "n"(N) : "memory"); }
template <int N> DI void wait_gr(u32x2 (&g)[4]) {
  asm volatile("s_waitcnt vmcnt(%4)" : "+v"(g[0]), "+v"(g[1]), "+v"(g[2]), "+v"(g[3]) : "n"(N) : "memory");
}
constexpr int SPIN_MAX = 1 << 16;
DI bool marked(u32x4 v) { return __any(((v[0] & v[1] & v[2] & v[3]) & 1u) == 0u); }
DI u32 no_mark(u32 v) { return v | 1u; }
DI void ld16_until_valid(u32x4& d, u32x4 srd, u32 voff) {
  if (__builtin_expect(marked(d), 0)) {
    int spin = SPIN_MAX;
    do {
      asm volatile("buffer_load_dwordx4 %0, %1, %2, 0 offen sc1\n\ts_waitcnt vmcnt(0)" : "+v"(d) : "v"(voff), "s"(srd) : "memory");
    } while (marked(d) && --spin);
  }
}
DI void pin4(f4& v) { asm volatile("" : "+v"(v)); }
DI void pin1(u32& v) { asm volatile("" : "+v"(v)); }
DI void pin(u32x2& v) { asm volatile("" : "+v"(v)); }
template <int N> DI void wait_lgkm() { __builtin_amdgcn_s_waitcnt(0xC07F | (N << 8)); SB(); }
DI u32x4 lds_rd(u32 a) { return *(const LDS_AS u32x4*)(size_t)a; }
template <int N> DI void wait_vm() {
  asm volatile("s_waitcnt vmcnt(%0)" ::"n"(N) : "memory");
  if (N == 0) __builtin_amdgcn_s_waitcnt(0x0F70);
}
DI void pf_line(const void* sbase, u32 voff, u32 lds) {
  asm volatile("s_mov_b32 m0, %2\n\ts_nop 0\n\tglobal_load_lds_dword %0, %1" ::"v"(voff), "s"(sbase), "s"(lds) : "memory");
}
DI void lgkm0() { asm volatile("" ::: "memory"); __builtin_amdgcn_s_waitcnt(0xC07F); asm volatile("" ::: "memory"); }
DI void bar() { asm volatile("" ::: "memory"); __builtin_amdgcn_s_barrier(); asm volatile("" ::: "memory"); }
DI int uni(int v) { return __builtin_amdgcn_readfirstlane(v); }
DI u32 lds_addr(const char* p) { return (u32)(size_t)(const LDS_AS char*)p; }

DI f2 ln_scalars(float mean, float var) {
  float m = rbf(mean);
  float v = rbf(var);
  float den = rbf(__builtin_amdgcn_sqrtf(rbf(v + 1e-5f)));
  return f2{m, __builtin_amdgcn_rcpf(den)};
}
DI u32 ln_pair(u32 v, float m0, float r0, float m1, float r1) {
  u32 d = pk_bf(bf_lo(v) - m0, bf_hi(v) - m1);
  return pk_bf(bf_lo(d) * r0, bf_hi(d) * r1);
}
DI f2 gelu2f(u32 p) {
  const float c1 = -2.0f * 0.7978845608028654f * 1.4426950408889634f, c2 = c1 * 0.044715f;
  const f2 u = {bf_lo(p), bf_hi(p)};
  const f2 t = u * (u * u * f2{c2, c2} + f2{c1, c1});
  const f2 d = f2{__builtin_amdgcn_exp2f(t.x), __builtin_amdgcn_exp2f(t.y)} + f2{1.f, 1.f};
  return u * f2{__builtin_amdgcn_rcpf(d.x), __builtin_amdgcn_rcpf(d.y)};
}
DI u32 gelu2(u32 p) { f2 o = gelu2f(p); return pk_bf(o.x, o.y); }
DI u32x2 gelu4(f4 v) { return u32x2{gelu2(pk_bf(v[0], v[1])), gelu2(pk_bf(v[2], v[3]))}; }
DI f2 row16_reduce_scatter(f4 a, f4 b) {
  float a01, a23, b01, b23, ra, rb;
  asm volatile("s_nop 1\n\t"
      "v_add_f32_dpp %0, %6, %6 row_ror:8 row_mask:0xf bank_mask:0x3\n\t"
      "v_add_f32_dpp %1, %7, %7 row_ror:8 row_mask:0xf bank_mask:0x3\n\t"
      "v_add_f32_dpp %2, %10, %10 row_ror:8 row_mask:0xf bank_mask:0x3\n\t"
      "v_add_f32_dpp %3, %11, %11 row_ror:8 row_mask:0xf bank_mask:0x3\n\t"
      "v_add_f32_dpp %0, %8, %8 row_ror:8 row_mask:0xf bank_mask:0xc\n\t"
      "v_add_f32_dpp %1, %9, %9 row_ror:8 row_mask:0xf bank_mask:0xc\n\t"
      "v_add_f32_dpp %2, %12, %12 row_ror:8 row_mask:0xf bank_mask:0xc\n\t"
      "v_add_f32_dpp %3, %13, %13 row_ror:8 row_mask:0xf bank_mask:0xc\n\t"
      "v_add_f32_dpp %4, %0, %0 row_half_mirror row_mask:0xf bank_mask:0x5\n\t"
      "v_add_f32_dpp %4, %1, %1 row_half_mirror row_mask:0xf bank_mask:0xa\n\t"
      "v_add_f32_dpp %5, %2, %2 row_half_mirror row_mask:0xf bank_mask:0x5\n\t"
      "v_add_f32_dpp %5, %3, %3 row_half_mirror row_mask:0xf bank_mask:0xa\n\t"
      "s_nop 0\n\t"
      "v_add_f32_dpp %4, %4, %4 quad_perm:[1,0,3,2] row_mask:0xf bank_mask:0xf\n\t"
      "v_add_f32_dpp %5, %5, %5 quad_perm:[1,0,3,2] row_mask:0xf bank_mask:0xf\n\t"
      "s_nop 0\n\t"
      "v_add_f32_dpp %4, %4, %4 quad_perm:[2,3,0,1] row_mask:0xf bank_mask:0xf\n\t"
      "v_add_f32_dpp %5, %5, %5 quad_perm:[2,3,0,1] row_mask:0xf bank_mask:0xf"
      : "=&v"(a01), "=&v"(a23), "=&v"(b01), "=&v"(b23), "=&v"(ra), "=&v"(rb)
      : "v"(a[0]), "v"(a[1]), "v"(a[2]), "v"(a[3]), "v"(b[0]), "v"(b[1]), "v"(b[2]), "v"(b[3]));
  return f2{ra, rb};
}

constexpr int K1_NWIN = 68, K1_NFILL = 9;
DI void tr_load(u32x2 (&v)[8], const char* base, u32 voff, u32 stride) {
#pragma unroll
  for (int i = 0; i < 8; ++i)
    asm volatile("global_load_dwordx2 %0, %1, %2" : "=v"(v[i]) : "v"(voff), "s"(base + (size_t)i * stride) : "memory");
}
DI void tr_deal(u32x2 (&v)[8], u32 lds, int lane, u32x4 (&o)[4]) {
  asm volatile("" : "+v"(v[0]), "+v"(v[1]), "+v"(v[2]), "+v"(v[3]), "+v"(v[4]), "+v"(v[5]), "+v"(v[6]), "+v"(v[7]));
#pragma unroll
  for (int c = 0; c < 4; ++c) {
    u32x4 t;
#pragma unroll
    for (int i = 0; i < 4; ++i)
      t[i] = __builtin_amdgcn_perm(v[2 * i + 1][c >> 1], v[2 * i][c >> 1], (c & 1) ? 0x07060302u : 0x05040100u);
    *(LDS_AS u32x4*)(size_t)(lds + lane * 64 + ((c ^ (lane >> 1)) & 3) * 16) = t;
  }
  lgkm0();
#pragma unroll
  for (int k = 0; k < 4; ++k) {
    const int q = k * 64 + lane;
    o[k] = *(const LDS_AS u32x4*)(size_t)(lds + (q >> 2) * 64 + ((q ^ (q >> 3)) & 3) * 16);
  }
}

constexpr int K1_TAB = 0, K1_XN = 12288, K1_STG = 24576, K1_LDS = 24576 + 8 * 328 * 4;
extern "C" __global__ __launch_bounds__(512) void k1_fwd(const u32* __restrict__ x, const u32x4* __restrict__ wr,
                                                         const u32x4* __restrict__ wi, const char* __restrict__ w_in,
                                                         char* __restrict__ ws) {
  extern __shared__ __attribute__((aligned(16))) char sm[];
  const int tid = threadIdx.x, lane = tid & 63, w = uni(tid >> 6);
  const int n = lane & 15, g = lane >> 4;
  const int b = blockIdx.x & 255;
  if (blockIdx.x >= 512) {
    const int q = blockIdx.x - 512;
    if (q < K1_NWIN) {
      const int task = q * 8 + w;
      if (task < 540) {
        const int it = task * 64 + lane, p = it / 432;
        u32x2 tv[8];
        u32x4 o[4];
        tr_load(tv, w_in, p * (8 * F * 2) + (it - p * 432) * 8, F * 2);
        wait_vm<0>();
        tr_deal(tv, lds_addr(sm) + w * 4096, lane, o);
#pragma unroll
        for (int k = 0; k < 4; ++k) ((u32x4*)(ws + WS_WIN))[task * 256 + k * 64 + lane] = o[k];
      }
    } else {
      const int z_ = q - K1_NWIN;
      u32x4* st = (u32x4*)(ws + WS_STAT) + z_ * 2048 + tid;
      u32 fv = z_ < 8 ? ~0u : 0u;
      u32x4 f = {fv, fv, fv, fv};
      st[0] = f; st[512] = f;
      if (z_ < 8) { st[1024] = f; st[1536] = f; }
    }
    return;
  }
  const int rg = (b & 7) * 32 + (b >> 3);
  if (blockIdx.x >= 256) {
    if (w == 7 && b < 64) {
      u64 c0, c1;
      asm volatile("s_getpc_b64 s[40:41]\n\ts_add_u32 s40, s40, k1_fwd@rel32@lo+4\n\ts_addc_u32 s41, s41, k1_fwd@rel32@hi+12\n\ts_mov_b64 %0, s[40:41]"
                   : "=s"(c0)::"scc", "s40", "s41");
      asm volatile("s_getpc_b64 s[40:41]\n\ts_add_u32 s40, s40, k_text_end@rel32@lo+4\n\ts_addc_u32 s41, s41, k_text_end@rel32@hi+12\n\ts_mov_b64 %0, s[40:41]"
                   : "=s"(c1)::"scc", "s40", "s41");
      const u32 len = c1 > c0 ? (u32)(c1 - c0) : 0u, off = ((b >> 3) * 64 + lane) * 128;
      pf_line((const void*)c0, off < len ? off : 0u, lds_addr(sm) + 14336);
      if (b < 16) pf_line(&g_tab_inv, ((b >> 3) * 48 + (lane < 48 ? lane : 0)) * 128, lds_addr(sm) + 14336);
      else if (b < 24) pf_line(&g_tab_col, lane * 128, lds_addr(sm) + 14336);
    }
    u32 mx = 0;
    const int tc = tid < KT ? tid : KT - 1;
    u32x4 a = wr[(size_t)rg * KT + tc], c = wi[(size_t)rg * KT + tc];
    if (tid < KT) {
      *(u32x4*)(sm + tid * 16) = a;
      *(u32x4*)(sm + 5136 + tid * 16) = c;
#pragma unroll
      for (int i = 0; i < 4; ++i) {
        mx = umax(mx, umax(a[i] & 0x7fffu, (a[i] >> 16) & 0x7fffu));
        mx = umax(mx, umax(c[i] & 0x7fffu, (c[i] >> 16) & 0x7fffu));
      }
    }
    mx = wave_umax(mx);
    u32* red = (u32*)(sm + 10272);
    if (lane == 0) red[w] = mx;
    lgkm0(); bar();
#pragma unroll
    for (int rep = 0; rep < 2; ++rep) {
      if (rep == 0 || tid < 2 * KT - 512) {
        const u16* s16 = (const u16*)sm + (tid >> 1) + rep * 256 + (tid & 1) * 4 * KT;
        u32x4 o;
#pragma unroll
        for (int r = 0; r < 4; ++r) o[r] = (u32)s16[r * KT] | ((u32)s16[2568 + r * KT] << 16);
        *(u32x4*)(g_spec + SP_WT + rg * 32 + (u32)(((tid >> 1) + rep * 256) * (TS * 4) + (tid & 1) * 16)) = o;
      }
    }
    if (tid == 448) {
      u32 m = 0;
#pragma unroll
      for (int i = 0; i < 8; ++i) m = umax(m, red[i]);
      ((u32*)(g_spec + SP_WMAX))[rg] = m;
    }
    wait_vm<0>();
    return;
  }
  const int s = rg * 8 + w;
  u32 xv[5];
#pragma unroll
  for (int i = 0; i < 5; ++i) xv[i] = x[(size_t)s * 320 + i * 64 + lane];
  glds(&g_tab_fwd.v[w][0][0], lane * 16, lds_addr(sm) + K1_TAB + w * 1024);
  if (w < 4) glds(&g_tab_fwd.v[8 + w][0][0], lane * 16, lds_addr(sm) + K1_TAB + (8 + w) * 1024);
  u32 sa[2][4];
#pragma unroll
  for (int nt = 0; nt < 2; ++nt) {
    const int k1 = 16 * nt + n;
#pragma unroll
    for (int r = 0; r < 4; ++r) {
      int k2 = 4 * g + r, k = k1 + 40 * k2;
      int kt = g < 2 ? k : 640 - k;
      bool ok = k1 <= 20 && !(k1 == 0 && k2 > 8) && !(k1 == 20 && k2 >= 8);
      sa[nt][r] = lds_addr(sm) + K1_STG + (w * 328 + (ok ? kt : 324)) * 4;
      asm volatile("" : "+v"(sa[nt][r]));
    }
  }
  u32 dsto = (tid >> 1) * (TS * 4) + (tid & 1) * 16, srco = lds_addr(sm) + K1_STG + ((tid & 1) * 4 * 328 + (tid >> 1)) * 4;
  asm volatile("" : "+v"(dsto), "+v"(srco));
  wait_vm<0>();
  bar();
  u32x4 tab[12];
#pragma unroll
  for (int i = 0; i < 12; ++i) tab[i] = lds_rd(lds_addr(sm) + K1_TAB + i * 1024 + lane * 16);
  SB();
  const float pilot = bf_lo((u32)uni((int)xv[0]));
  f2 s2 = {0.f, 0.f}, q2 = {0.f, 0.f};
#pragma unroll
  for (int i = 0; i < 5; ++i) {
    f2 d = f2{bf_lo(xv[i]), bf_hi(xv[i])} - pilot;
    s2 += d;
    q2 += d * d;
  }
  const f2 mq = wave_sum2(opq(s2.x) + opq(s2.y), opq(q2.x) + opq(q2.y)) * (1.0f / 640.0f);
  const f2 ln = ln_scalars(pilot + mq.x, __builtin_fmaxf(mq.y - mq.x * mq.x, 0.f));
  char* xn = sm + K1_XN + w * 1536;
#pragma unroll
  for (int i = 0; i < 5; ++i) {
    u32 o = ln_pair(xv[i], ln.x, ln.y, ln.x, ln.y);
    *(u32*)(xn + (i * 64 + lane) * 4) = pk_h(bf_lo(o), bf_hi(o));
  }
  *(u32*)(xn + (320 + lane) * 4) = 0u;
  lgkm0();
  u32x2 a0 = tr_read(xn + lane * 8), a1 = tr_read(xn + 512 + lane * 8), a2 = tr_read(xn + 1024 + lane * 8);
  u32x4 af = {a0[0], a0[1], a1[0], a1[1]};
  const u32x4 tre = tab[4], tim = tab[5], fr = tab[10], fi = tab[11];
  const f4 z = {0.f, 0.f, 0.f, 0.f};
  f4 ore[2], oim[2];
#pragma unroll
  for (int nt = 0; nt < 2; ++nt) {
    f4 re = mfma_h(af, tab[nt], z);
    f4 im = mfma_h(af, tab[2 + nt], z);
    const u32x4 at = nt ? u32x4{0u, 0u, a2[0], a2[1]} : u32x4{a2[0], a2[1], 0u, 0u};
    re = mfma_h(at, tre, re);
    im = mfma_h(at, tim, im);
    u32x4 fb_ = cmul_pk(re, im, __builtin_bit_cast(f4, tab[6 + 2 * nt]), __builtin_bit_cast(f4, tab[7 + 2 * nt]));
    ore[nt] = mfma_h(fr, fb_, z);
    oim[nt] = mfma_h(fi, fb_, z);
  }
#pragma unroll
  for (int nt = 0; nt < 2; ++nt)
#pragma unroll
    for (int r = 0; r < 4; ++r) *(LDS_AS u32*)(size_t)sa[nt][r] = pk_h(ore[nt][r], oim[nt][r]) & ~1u;
  __syncthreads();
  {
    const LDS_AS u32* sp = (const LDS_AS u32*)(size_t)srco;
    u32x4 o0 = {sp[0], sp[328], sp[2 * 328], sp[3 * 328]};
    *(u32x4*)(g_spec + SP_T + rg * 32 + dsto) = o0;
    if (tid < 2 * KT - 512) {
      u32x4 o1 = {sp[256], sp[256 + 328], sp[256 + 2 * 328], sp[256 + 3 * 328]};
      *(u32x4*)(g_spec + SP_T + rg * 32 + 256 * (TS * 4) + dsto) = o1;
    }
  }
}

constexpr int SLOT = 16384, NU = 8;
constexpr int RED = NU * SLOT, MK_LDS = RED + 32768;
constexpr int P2_BASE = 65536, P2_EX2 = 10240, P2_W = 22528, P2_SIZE = 30720, P2_TAB = P2_BASE + 2 * P2_SIZE;
constexpr int P3_TAB = 147456, P3_Z = 110592, P3_ZSTR = 420, P3_OUT = 124928;
constexpr int G3_RED = 138240;

DI void st16_dev(__amdgpu_buffer_rsrc_t r, u32 off, u32x4 v) { __builtin_amdgcn_raw_buffer_store_b128(v, r, off, 0, 16); }
DI void st8_dev(__amdgpu_buffer_rsrc_t r, u32 off, u32x2 v) { __builtin_amdgcn_raw_buffer_store_b64(v, r, off, 0, 16); }
DI u32x2 ld8_dev(__amdgpu_buffer_rsrc_t r, u32 off) { return __builtin_amdgcn_raw_buffer_load_b64(r, off, 0, 16); }
DI void set_flag(char* ws, int off) {
  __hip_atomic_store((GLB_AS u32*)(ws + WS_FLAG + off), 1u, __ATOMIC_RELAXED, __HIP_MEMORY_SCOPE_AGENT);
}
DI void poll_flags(char* ws, int off, int last, int lane) {
  const GLB_AS u32* p = (const GLB_AS u32*)(ws + WS_FLAG + off) + (lane < last ? lane : last);
  for (int spin = 0; spin < SPIN_MAX; ++spin) {
    u32 v = __hip_atomic_load(p, __ATOMIC_RELAXED, __HIP_MEMORY_SCOPE_AGENT);
    if (__all(v != 0u)) break;
  }
  asm volatile("" ::: "memory");
}

DI void ld_x(u32x2 (&xv)[3], u32& braw, const char* xrow, const char* bsp, int n, int g, int srow) {
  asm volatile("global_load_dwordx2 %0, %3, %5\n\tglobal_load_dwordx2 %1, %3, %5 offset:512\n\tglobal_load_dwordx2 %2, %4, %5 offset:1024"
               : "=&v"(xv[0]), "=&v"(xv[1]), "=&v"(xv[2]) : "v"(n * 32 + g * 8), "v"((n & 7) * 32 + g * 8), "s"(xrow) : "memory");
  asm volatile("global_load_ushort %0, %1, %2" : "=&v"(braw) : "v"((srow + n) * 2), "s"(bsp) : "memory");
}
__global__ __launch_bounds__(512, 2) void k2_mega(const char* __restrict__ x, const char* __restrict__ wsp,
                                                  const char* __restrict__ bsp, const char* __restrict__ w_out,
                                                  char* __restrict__ ws,
                                                  u32x2* __restrict__ out) {
  extern __shared__ __attribute__((aligned(16))) char sm[];
  const int tid = threadIdx.x, lane = tid & 63, w = uni(tid >> 6);
  const int n = lane & 15, g = lane >> 4;
  const int xc = blockIdx.x & 7, bi = blockIdx.x >> 3;
  const int rt = (xc >> 1) * 8 + (bi >> 2), cg = (xc & 1) * 4 + (bi & 3);
  const int nt_all = (cg & 3) == 3 ? 6 : 7, t0 = cg * 7 - (cg >> 2);
  const int mt = w & 3, h = w >> 2;
  const int ncnt = h ? nt_all - 4 : 4, nfirst = 4 * h;
  const int srow = rt * 64 + mt * 16;
  const u32 smb = lds_addr(sm);
  const __amdgpu_buffer_rsrc_t wsr = __builtin_amdgcn_make_buffer_rsrc(ws, 0, (int)WS_END, 0x00020000);
  const f4 z = {0.f, 0.f, 0.f, 0.f};
  const u32 l16 = lane * 16;

  const u32x4 srd = {(u32)(size_t)ws, (u32)((size_t)ws >> 32) & 0xffffu, (u32)WS_END, 0x00020000u};
  const u32x4 srd_t = {(u32)(size_t)g_spec, (u32)((size_t)g_spec >> 32) & 0xffffu, (u32)SP_WT, 0x00020000u};
  const __amdgpu_buffer_rsrc_t tsr = __builtin_amdgcn_make_buffer_rsrc(g_spec, 0, (int)SP_WT, 0x00020000);
  const u32 l16b = (lane < nt_all * 16 - 64 ? lane : nt_all * 16 - 65) * 16;
  const u32 ldw = smb + w * 2048;
  const u32 winb = (u32)WS_WIN + ((8 * h + mt) * F + t0 * 16) * 16;
  auto g1_issue = [&](auto U, int q) {
    constexpr int u = U.value, v = u < 10 ? u : u - 10;
    const u32 so = winb + ((v >> 1) * 16 + (v & 1) * 4) * (F * 16) + (u < 10 ? FH * 16 : 0);
    if (q == 0) bdma0<(u % NU) * SLOT, false>(srd, l16, so, ldw);
    else bdma1<false>(srd, l16b, so);
  };
  const u32 zb = (u32)WS_Z2 + ((8 * h + mt) * FH + t0 * 16) * 16;
  auto g2_issue = [&](auto U, int q) {
    constexpr int u = U.value;
    const u32 so = zb + ((u >> 1) * 16 + (u & 1) * 4) * (FH * 16);
    if (q == 0) bdma0<(u % NU) * SLOT, true>(srd, l16, so, ldw);
    else bdma1<true>(srd, l16b, so);
  };
  const u32 wob = (u32)WS_WOUT + cg * 138240 + w * 2048;
  auto wo_issue = [&](auto V, int q) {
    if (q == 0) bdma0<V.value * SLOT, true>(srd, l16, wob + V.value * SLOT, ldw);
    else bdma1<true>(srd, l16, wob + V.value * SLOT);
  };
  u32 fbA0 = smb + (4 * h + g) * 2048 + n * 16 + h * 1024, fbB0 = fbA0 - h * 1792;
  u32 fbA1 = fbA0 + 65536, fbB1 = fbB0 + 65536;
  asm("" : "+v"(fbA1), "+v"(fbB0), "+v"(fbB1));
  auto frag = [&](auto U, int j) {
    constexpr int sl = U.value % NU;
    return lds_rd((j < 3 ? (sl < 4 ? fbA0 : fbA1) : (sl < 4 ? fbB0 : fbB1)) + (sl & 3) * SLOT + j * 256);
  };
  const u32 hoff = ((u32)(rt * 8 + 2 * mt + (n >> 3)) * 80) * 128 + (n & 7) * 16;
  float biasv;
  u32x4 wf[32];
  const u32 wvo = (lane >> 3) * 4096 + (((lane & 7) ^ (lane >> 4)) << 4);
  const char* wsb = wsp + (size_t)srow * 4096 + h * 128;
  auto wsp_issue = [&](int i, int half) {
    const int sl = i < 8 ? (i & 3) : (i < 14 ? (i + 4) % NU : i - 6);
    glds(wsb + i * 256 + half * 32768, wvo ^ (half << 6), ldw + sl * SLOT + half * 1024);
  };
  u32 wr0 = ldw + n * 128 + ((g ^ (n >> 1)) << 4), wr1 = wr0 + 65536;
  asm("" : "+v"(wr1));
  auto wsp_read = [&](int i) {
    const int sl = i < 8 ? (i & 3) : (i < 14 ? (i + 4) % NU : i - 6);
    const u32 a = sl < 4 ? wr0 : (sl < 8 ? wr1 : wr1 + 65536);
    wf[2 * i] = lds_rd(a + (sl & 3) * SLOT);
    wf[2 * i + 1] = lds_rd((a ^ 64) + (sl & 3) * SLOT);
  };
#define R2(k) do { SB(); wsp_issue(4 + ((k) >> 1), (k) & 1); SB(); } while (0)

  const int rg = rt * 8 + cg;
  u32x2 xv[3];
  u32 braw;
  float winv;
  const int task = blockIdx.x < KT - 256 || w < 3 || w > 4 ? 1000 : ((int)blockIdx.x - (KT - 256)) + 191 * (w - 3);
  u32x2 tv[8];
  {
    const bool two = blockIdx.x < KT - 256;
    const u32 so = ((g & 1) * 1024 + 32 * w + 16 * (g >> 1) + n) * 4;
    const char* t1 = g_spec + SP_T + (size_t)blockIdx.x * (TS * 4);
    const char* wt = g_spec + SP_WT + (size_t)blockIdx.x * (TS * 4);
    const u32 pb = smb + P2_BASE, wo_ = ((tid & 255) * 2 + (tid >> 8)) * 16;
    u32 ua[4], ub[4];
    asm volatile("global_load_dword %0, %4, %5\n\tglobal_load_dword %1, %4, %5 offset:1024\n\t"
                 "global_load_dword %2, %4, %5 offset:2048\n\tglobal_load_dword %3, %4, %5 offset:3072"
                 : "=&v"(ua[0]), "=&v"(ua[1]), "=&v"(ua[2]), "=&v"(ua[3]) : "v"(so), "s"(t1) : "memory");
    if (two) {
      asm volatile("global_load_dword %0, %4, %5\n\tglobal_load_dword %1, %4, %5 offset:1024\n\t"
                   "global_load_dword %2, %4, %5 offset:2048\n\tglobal_load_dword %3, %4, %5 offset:3072"
                   : "=&v"(ub[0]), "=&v"(ub[1]), "=&v"(ub[2]), "=&v"(ub[3]) : "v"(so), "s"(t1 + 256 * (TS * 4)) : "memory");
      glds(wt + 256 * (TS * 4), wo_, pb + P2_SIZE + P2_W + w * 1024);
    }
    glds(wt, wo_, pb + P2_W + w * 1024);
    glds(w ? (const char*)&g_tab_col.v[w][0][0] : g_spec + SP_WMAX, l16, smb + P2_TAB + w * 1024);
    u32x4 f8a, f8b;
    {
      const float o8 = (float)(2 * (n >> 2) + (n & 1)) * 0.125f, a0 = (float)(4 * (g & 1)), q = (n & 2) ? 0.25f : 0.f;
#pragma unroll
      for (int i = 0; i < 4; ++i) {
        const float an = __builtin_amdgcn_fractf(o8 * (a0 + i) + q);
        const u32 v = pk_h(__builtin_amdgcn_cosf(an), __builtin_amdgcn_sinf(an));
        f8a[i] = g < 2 ? v : 0u;
        f8b[i] = g < 2 ? 0u : v;
      }
    }
    f4 tax, tay, t3x, t3y;
    {
      const float fb_ = (float)(32 * w + n) * (-1.0f / 2048.0f), fj = (float)(2 * g);
      const float fw = (float)w * (1.0f / 2048.0f), f3 = (float)(64 * g + n);
#pragma unroll
      for (int i = 0; i < 4; ++i) {
        float a0 = (fb_ - (float)(i >> 1) * (1.0f / 128.0f)) * (fj + (float)(i & 1)), a3 = fw * (f3 + 16.f * i);
        tax[i] = __builtin_amdgcn_cosf(a0); tay[i] = __builtin_amdgcn_sinf(a0);
        t3x[i] = __builtin_amdgcn_cosf(a3); t3y[i] = __builtin_amdgcn_sinf(a3);
      }
    }
    u32 ex1w = pb + (2 * g * 320 + n * 20 + 2 * w) * 4, ex1r = pb + (w * 320 + n * 20 + 4 * g) * 4;
    u32 wrd = pb + P2_W + ((w >> 2) * 256 + n + 64 * g) * 16 + (w & 3) * 4;
    u32 ex2w = pb + P2_EX2 + ((64 * g + n) * 12 + w) * 4;
    u32 ex2r = pb + P2_EX2 + ((32 * w + 16 * (g >> 1) + n) * 12 + 4 * (g & 1)) * 4;
    u32 tbr = smb + P2_TAB + l16, t2o = (u32)SP_T + blockIdx.x * (TS * 4) + (lane >> 3) * 1024 + w * 128 + (lane & 7) * 16;
    u32 otw = pb + w * 1024 + (g * 64 + n) * 4;
    asm volatile("" : "+v"(tax), "+v"(tay), "+v"(t3x), "+v"(t3y), "+v"(f8a), "+v"(f8b));
    asm volatile("" : "+v"(ex1w), "+v"(ex1r), "+v"(wrd), "+v"(ex2w), "+v"(ex2r), "+v"(tbr), "+v"(t2o), "+v"(otw));
    SB();
    const u32x4 zu = {0u, 0u, 0u, 0u};
    const bool lo = g < 2;
    asm volatile("s_waitcnt vmcnt(0)" : "+v"(ua[0]), "+v"(ua[1]), "+v"(ua[2]), "+v"(ua[3]), "+v"(ub[0]), "+v"(ub[1]), "+v"(ub[2]), "+v"(ub[3])::"memory");
    auto stage1 = [&](u32 (&u)[4], int off) {
      const u32x4 uv = {u[0], u[1], u[2], u[3]};
      f4 d0 = mfma_h(f8a, uv, z), d1 = mfma_h(f8b, uv, z);
      u32x4 o = cmul_pk(f4{d0[0], d0[1], d1[0], d1[1]}, f4{d0[2], d0[3], d1[2], d1[3]}, tax, tay);
      *(LDS_AS u32x2*)(size_t)(ex1w + off) = u32x2{o[0], o[2]};
      *(LDS_AS u32x2*)(size_t)(ex1w + off + 1280) = u32x2{o[1], o[3]};
    };
    stage1(ua, 0);
    if (two) stage1(ub, P2_SIZE);
    pf_line(wsp + (size_t)(rg * 8 + w) * 4096, (lane & 31) * 128, smb + MK_LDS - 1024);
    lgkm0(); bar();
    u32x4 fa[2], wv[2];
    fa[0] = lds_rd(ex1r);
    const u32x4 Fr = lds_rd(tbr + 4096), Fi = lds_rd(tbr + 5120);
    fa[1] = lds_rd(ex1r + P2_SIZE);
    const f4 t1x = __builtin_bit_cast(f4, lds_rd(tbr + 2048)), t1y = __builtin_bit_cast(f4, lds_rd(tbr + 3072));
    const u32x4 wm4 = lds_rd(tbr);
#pragma unroll
    for (int r = 0; r < 8; ++r) wv[r >> 2][r & 3] = *(const LDS_AS u32*)(size_t)(wrd + (r >> 2) * P2_SIZE + (r & 3) * 256);
    asm volatile("" ::: "memory");
    const u32x4 Gr = lds_rd(tbr + 6144), Gi = lds_rd(tbr + 7168), g8 = lds_rd(tbr + 1024);
    SB();
    int wex = (int)(wave_umax(umax(umax(wm4[0], wm4[1]), umax(wm4[2], wm4[3]))) >> 7);
    wex = wex < 4 ? 4 : (wex > 240 ? 240 : wex);
    const float wk = as_f((u32)(245 - wex) << 23);
    winv = as_f((u32)(wex - 2) << 23);
    auto chain = [&](auto NC) {
      constexpr int nc = NC.value;
      f4 wx[nc], wy[nc], re[nc], im[nc];
#pragma unroll
      for (int c = 0; c < nc; ++c) { re[c] = mfma_h(fa[c], Fr, z); im[c] = mfma_h(fa[c], Fi, z); }
#pragma unroll
      for (int c = 0; c < nc; ++c) {
#pragma unroll
        for (int r = 0; r < 4; ++r) { wx[c][r] = bf_lo(wv[c][r]); wy[c][r] = bf_hi(wv[c][r]); }
        wx[c] *= wk; wy[c] *= wk;
      }
#pragma unroll
      for (int c = 0; c < nc; ++c) {
        fa[c] = cmul_pk(re[c], im[c], t1x, -t1y);
        re[c] = mfma_h(Fr, fa[c], z); im[c] = mfma_h(Fi, fa[c], z);
      }
#pragma unroll
      for (int c = 0; c < nc; ++c) {
        fa[c] = cmul_pk(re[c], im[c], wx[c], wy[c]);
        re[c] = mfma_h(fa[c], Gr, z); im[c] = mfma_h(fa[c], Gi, z);
      }
#pragma unroll
      for (int c = 0; c < nc; ++c) {
        fa[c] = cmul_pk(re[c], im[c], t1x, t1y);
        re[c] = mfma_h(Gr, fa[c], z); im[c] = mfma_h(Gi, fa[c], z);
      }
#pragma unroll
      for (int c = 0; c < nc; ++c) {
        fa[c] = cmul_pk(re[c], im[c], t3x, t3y);
#pragma unroll
        for (int r = 0; r < 4; ++r) *(LDS_AS u32*)(size_t)(ex2w + c * P2_SIZE + r * 768) = fa[c][r];
      }
    };
    if (two) chain(IC<2>{}); else chain(IC<1>{});
    lgkm0(); bar();
    auto stage3 = [&](auto NC) {
      constexpr int nc = NC.value;
#pragma unroll
      for (int c = 0; c < nc; ++c) {
        u32x4 v = lds_rd(ex2r + c * P2_SIZE);
        f4 d0 = mfma_h(lo ? g8 : zu, v, z), d1 = mfma_h(lo ? zu : g8, v, z);
        LDS_AS u32* o = (LDS_AS u32*)(size_t)(otw + c * P2_SIZE);
        o[0] = no_mark(pk_h(d0[0], d0[2])); o[16] = no_mark(pk_h(d1[0], d1[2]));
        o[32] = no_mark(pk_h(d0[1], d0[3])); o[48] = no_mark(pk_h(d1[1], d1[3]));
      }
      lgkm0();
#pragma unroll
      for (int c = 0; c < nc; ++c) st16_dev(tsr, t2o + c * (256 * TS * 4), lds_rd(pb + w * 1024 + l16 + c * P2_SIZE));
    };
    if (two) stage3(IC<2>{}); else stage3(IC<1>{});
    if (task < 270) {
      const int it = task * 64 + lane, p = it / 160;
      tr_load(tv, w_out, p * (8 * D * 2) + (it - p * 160) * 8, D * 2);
    }
    ld_x(xv, braw, x + (size_t)(rg * 8 + w) * 1280, bsp, n, g, srow);
    glds(&g_tab_inv.v[w][0][0], l16, smb + P3_TAB + w * 1024);
    glds(&g_tab_inv.v[8 + (w & 3)][0][0], l16, smb + P3_TAB + (8 + (w & 3)) * 1024);
    lgkm0();
#pragma unroll
    for (int q = 0; q < 8; ++q) wsp_issue(q >> 1, q & 1);
    bar();
  }

  {
    u32x4 z0, z1;
    const int k2 = (tid >> 1) & 15, ka = (tid >> 5) + 40 * k2, kb = (tid < 160 ? 16 + (tid >> 5) : 20) + 40 * k2;
    const u32 tb_ = (u32)SP_T + rg * 32 + (tid & 1) * 16;
    const u32 oa = tb_ + (k2 < 8 ? ka : 640 - ka) * (TS * 4), ob_ = tb_ + (k2 < 8 ? kb : 640 - kb) * (TS * 4);
    if (w < 3) ld16_async<0>(z1, srd_t, ob_, 0);
    ld16_async<0>(z0, srd_t, oa, 0);
    u32 zw = smb + P3_Z + ((tid >> 5) * 20 + ((tid >> 1) & 15) + (tid & 1) * 4 * P3_ZSTR) * 4;
    u32 zr0 = smb + P3_Z + (w * P3_ZSTR + n * 20 + 4 * g) * 4, zr1 = smb + P3_Z + (w * P3_ZSTR + (n < 4 ? 16 + n : 20) * 20 + 4 * g) * 4;
    u32 tr_ = smb + P3_TAB + l16;
    u32 ow = smb + P3_OUT + ((w ^ (n & 7)) << 4) + (g >> 1) * 128 + n * 256;
    u32 ow2 = n < 8 ? ow + 8192 + (g & 1) * 10240 : smb + MK_LDS - 1024 + l16;
    ow += (g & 1) * 4096;
    u32 go = (u32)rg * 10240 + (tid >> 3) * 128 + (((tid ^ (tid >> 4)) & 7) << 4);
    u32 or0 = smb + P3_OUT + tid * 16, or1 = or0 + (tid < 128 ? 8192 : (tid < 256 ? 16384 : 0));
    u32 go1 = tid < 128 ? (u32)WS_HN + go + 8192 : (tid < 256 ? (u32)WS_H + go + 8192 - 2048 : 0x7ffffff0u);
    asm volatile("" : "+v"(zw), "+v"(zr0), "+v"(zr1), "+v"(tr_), "+v"(ow), "+v"(ow2), "+v"(go), "+v"(or0), "+v"(or1), "+v"(go1));
    asm volatile("s_waitcnt vmcnt(0)" : "+v"(z0), "+v"(z1), "+v"(xv[0]), "+v"(xv[1]), "+v"(xv[2]), "+v"(braw)::"memory");
#pragma unroll
    for (int i = 0; i < 4; ++i) wsp_read(i);
    if (task < 270) {
      u32x4 o[4];
      tr_deal(tv, smb + 65536 + w * 4096, lane, o);
#pragma unroll
      for (int k = 0; k < 4; ++k) {
        const int e = task * 256 + k * 64 + lane, pp = e / 640, d = e - pp * 640, dg = d / 80;
        st16_dev(wsr, (u32)WS_WOUT + ((dg * 108 + pp) * 80 + (d - dg * 80)) * 16, o[k]);
      }
    }
    ld16_until_valid(z0, srd_t, oa);
#pragma unroll
    for (int i = 0; i < 4; ++i) *(LDS_AS u32*)(size_t)(zw + i * P3_ZSTR * 4) = z0[i];
    if (w < 3) {
      ld16_until_valid(z1, srd_t, ob_);
      if (tid < 160) {
#pragma unroll
        for (int i = 0; i < 4; ++i) *(LDS_AS u32*)(size_t)(zw + i * P3_ZSTR * 4 + 1280) = z1[i];
      }
    }
    biasv = bf_lo(braw);
    f4 xf[3];
#pragma unroll
    for (int nt = 0; nt < 3; ++nt) {
      xf[nt] = f4{bf_lo(xv[nt][0]), bf_hi(xv[nt][0]), bf_lo(xv[nt][1]), bf_hi(xv[nt][1])};
      asm volatile("" : "+v"(xf[nt]));
    }
    lgkm0(); bar();
    u32x4 fz[2], tb[12];
    fz[0] = lds_rd(zr0);
    tb[0] = lds_rd(tr_); tb[1] = lds_rd(tr_ + 1024);
    fz[1] = lds_rd(zr1);
#pragma unroll
    for (int i = 2; i < 12; ++i) tb[i] = lds_rd(tr_ + i * 1024);
    SB();
    static_for<4, 6>([&](auto U) { g1_issue(U, 0); g1_issue(U, 1); });
    u32x4 fv[2];
#pragma unroll
    for (int m = 0; m < 2; ++m) {
      f4 re = mfma_h(fz[m], tb[0], z), im = mfma_h(fz[m], tb[1], z);
      fv[m] = cmul_pk(re, im, __builtin_bit_cast(f4, tb[2 + 2 * m]), __builtin_bit_cast(f4, tb[3 + 2 * m]));
    }
    u32x2 hp[3];
    f4 yy[3];
    const bool v2 = n < 8;
#pragma unroll
    for (int nt = 0; nt < 3; ++nt) {
      yy[nt] = mfma_h(fv[0], tb[6 + 2 * nt], z);
      yy[nt] = mfma_h(fv[1], tb[7 + 2 * nt], yy[nt]);
    }
    lgkm0();
    R2(0);
#pragma unroll
    for (int nt = 0; nt < 3; ++nt) {
      f4 y = yy[nt] * winv;
      u32 y0 = pk_bf(y[0], y[1]), y1 = pk_bf(y[2], y[3]);
      hp[nt][0] = pk_bf(xf[nt][0] + bf_lo(y0), xf[nt][1] + bf_hi(y0));
      hp[nt][1] = pk_bf(xf[nt][2] + bf_lo(y1), xf[nt][3] + bf_hi(y1));
      R2(1 + nt);
    }
    const float pilot = bf_lo((u32)uni((int)hp[0][0]));
    f2 s2 = {0.f, 0.f}, q2 = {0.f, 0.f};
#pragma unroll
    for (int i = 0; i < 6; ++i) {
      u32 v = hp[i >> 1][i & 1];
      f2 d = f2{bf_lo(v), bf_hi(v)} - pilot;
      if (i >= 4) d = v2 ? d : f2{0.f, 0.f};
      s2 += d;
      q2 += d * d;
    }
    R2(4);
    *(LDS_AS u32x4*)(size_t)(ow + 10240) = widen(hp[0], hp[1]);
    R2(5);
    const f2 mq = wave_sum2(opq(s2.x) + opq(s2.y), opq(q2.x) + opq(q2.y)) * (1.0f / 640.0f);
    R2(6);
    const f2 ln = ln_scalars(pilot + mq.x, __builtin_fmaxf(mq.y - mq.x * mq.x, 0.f));
    R2(7);
    u32 dd[6];
#pragma unroll
    for (int i = 0; i < 6; ++i) dd[i] = pk_bf(bf_lo(hp[i >> 1][i & 1]) - ln.x, bf_hi(hp[i >> 1][i & 1]) - ln.x);
    SB();
    u32x2 on[3];
#pragma unroll
    for (int i = 0; i < 6; ++i) on[i >> 1][i & 1] = pk_bf(bf_lo(dd[i]) * ln.y, bf_hi(dd[i]) * ln.y);
    *(LDS_AS u32x4*)(size_t)ow = widen(on[0], on[1]);
    *(LDS_AS u32x4*)(size_t)ow2 = widen(on[2], hp[2]);
    lgkm0(); bar();
    u32x4 c0 = lds_rd(or0), c1 = lds_rd(or1), c2 = lds_rd(or0 + 10240);
    st16_dev(wsr, (u32)WS_HN + go, c0);
    st16_dev(wsr, go1, c1);
    wait_vm<2>();
#pragma unroll
    for (int i = 4; i < 8; ++i) wsp_read(i);
    wait_vm<0>();
    lgkm0(); bar();
    if (tid == 0) set_flag(ws, rt * 128 + cg * 4);
    st16_dev(wsr, (u32)WS_H + go, c2);
    static_for<0, 4>([&](auto U) { g1_issue(U, 0); g1_issue(U, 1); });
    static_for<6, 8>([&](auto U) { g1_issue(U, 0); g1_issue(U, 1); });
    if (w == 0) poll_flags(ws, rt * 128, 7, lane);
    wait_vm<8>();
    bar();
  }

  u32x2 z1p[4];
  f4 acc[7], fin[4];
  u32x4 fa[7];
  auto reduce_put = [&]() {
    f4* red = (f4*)(sm + RED) + w * 256 + lane;
#pragma unroll
    for (int j = 0; j < 4; ++j) red[j * 64] = acc[3 + j];
  };
  auto reduce_get = [&]() {
    const f4* red = (const f4*)(sm + RED) + (w ^ 4) * 256 + h * 64 + lane;
#pragma unroll
    for (int j = 0; j < 4; ++j) fin[j] = acc[j] + red[j * 64];
  };
  {
    u32x4 af[10];
    {
      const u32 ao = (u32)WS_HN + hoff + (8 * h + g) * 128;
      static_for<0, 10>([&](auto U) {
        constexpr int u = U.value;
        ld16_async<((u >> 1) & 1) * 2048 + (u & 1) * 512>(af[u], srd, ao, (u >> 2) * 4096);
      });
    }
#pragma unroll
    for (int j = 0; j < 7; ++j) fa[j] = frag(IC<0>{}, j);
    u32x2 z2p[4];
    u32x2 gr[4];
    const u32 stoff = (u32)WS_STAT + ((u32)(srow + 4 * g) * 16 + n) * 8;
    static_for<0, 20>([&](auto U) {
      constexpr int u = U.value;
      if constexpr (u < 10) {
        wait_vm_tie<((u & 1) && u >= 7) ? 6 : (u < 2 ? 9 - u : 5 + u)>(af[u]);
      } else if constexpr (u == 11 || u == 13) {
        wait_vm<6>();
      } else if constexpr (u == 15) {
        wait_vm<10>();
      } else if constexpr (u == 19) {
        wait_vm<6>();
      }
      if constexpr (u & 1) bar();
      if constexpr (u == 19) {
        if (tid == 0) set_flag(ws, 4096 + cg * 128 + rt * 4);
      }
      constexpr bool has_next = u + 1 < 20;
#pragma unroll
      for (int j = 0; j < 7; ++j) {
        if (j == 0) wait_lgkm<3>();
        if (j == 4) wait_lgkm<has_next ? 4 : 0>();
        if constexpr (u < 10) acc[j] = mfma_b(af[u], fa[j], u == 0 ? z : acc[j]);
        else acc[j] = mfma_b(fa[j], af[u - 10], u == 10 ? z : acc[j]);
        SB();
        if constexpr (has_next) fa[j] = frag(IC<u + 1>{}, j);
        if (j == 1 || j == 4) {
          if constexpr (u + 6 >= 20) wsp_issue(u - 6, j == 4);
          else if constexpr (u >= 2) g1_issue(IC<u + 6>{}, j == 4);
        }
        if constexpr (u == 13) {
          if (j >= 5) {
            asm volatile("global_load_dwordx2 %0, %1, %2 offset:%3 sc1" : "=v"(gr[2 * j - 10]) : "v"(stoff), "s"(ws), "n"((2 * j - 10) * 128) : "memory");
            asm volatile("global_load_dwordx2 %0, %1, %2 offset:%3 sc1" : "=v"(gr[2 * j - 9]) : "v"(stoff), "s"(ws), "n"((2 * j - 9) * 128) : "memory");
          }
        }
        SB();
      }
      if constexpr (u == 9) {
        reduce_put();
        lgkm0(); bar();
        reduce_get();
        f4 sum = z, sq = z;
#pragma unroll
        for (int j = 0; j < 4; ++j) {
          if (j < 2 || j < ncnt) {
            f2 a = gelu2f(pk_bf(fin[j][0], fin[j][1])), b = gelu2f(pk_bf(fin[j][2], fin[j][3]));
            z2p[j] = u32x2{pk_bf(a.x, a.y), pk_bf(b.x, b.y)};
            f4 zv = {a.x, a.y, b.x, b.y};
            sum += zv;
            sq += zv * zv;
          } else {
            z2p[j] = u32x2{0u, 0u};
          }
        }
        const f2 rs = row16_reduce_scatter(sum, sq);
        u32x2 gv = {as_u(rs.x), as_u(rs.y) & 0x7fffffffu};
        st8_dev(wsr, (n & 3) ? 0x7ffffff0u : (u32)WS_STAT + ((u32)(srow + (lane >> 2)) * 16 + cg * 2 + h) * 8, gv);
        SB();
      }
      if constexpr (u == 15) {
        wait_gr<4>(gr);
        bool ok = !((gr[0][1] | gr[1][1] | gr[2][1] | gr[3][1]) >> 31);
        for (int spin = 0; !__all(ok) && spin < SPIN_MAX; ++spin) {
          asm volatile("" ::: "memory");
#pragma unroll
          for (int r = 0; r < 4; ++r) gr[r] = ld8_dev(wsr, stoff + r * 128);
          ok = !((gr[0][1] | gr[1][1] | gr[2][1] | gr[3][1]) >> 31);
        }
        const f2 rs = row16_reduce_scatter(f4{as_f(gr[0][0]), as_f(gr[1][0]), as_f(gr[2][0]), as_f(gr[3][0])},
                                           f4{as_f(gr[0][1]), as_f(gr[1][1]), as_f(gr[2][1]), as_f(gr[3][1])});
        const float mean = rs.x * (1.0f / 864.0f);
        const f2 lq = ln_scalars(mean, __builtin_fmaxf(rs.y * (1.0f / 864.0f) - mean * mean, 0.f));
        const float m0 = dpp<0x150>(lq.x), m1 = dpp<0x154>(lq.x), m2 = dpp<0x158>(lq.x), m3 = dpp<0x15c>(lq.x);
        const float r0 = dpp<0x150>(lq.y), r1 = dpp<0x154>(lq.y), r2 = dpp<0x158>(lq.y), r3 = dpp<0x15c>(lq.y);
        u32x2 o[4];
#pragma unroll
        for (int j = 0; j < 4; ++j) o[j] = u32x2{ln_pair(z2p[j][0], m0, r0, m1, r1), ln_pair(z2p[j][1], m2, r2, m3, r3)};
        const u32 zo = (u32)WS_Z2 + ((u32)((srow >> 3) + (g >> 1)) * FH + (t0 + nfirst + (g & 1)) * 16 + n) * 16;
        st16_dev(wsr, zo, widen(o[0], o[1]));
        u32x4 w2 = widen(o[2], o[3]);
        st16_dev(wsr, 2 + (g & 1) < ncnt ? zo + 512 : 0x7ffffff0u, w2);
        SB();
      }
    });
    reduce_put();
    lgkm0(); bar();
    reduce_get();
#pragma unroll
    for (int j = 0; j < 2; ++j) {
      z1p[j] = gelu4(fin[j]);
      asm volatile("" : "+v"(z1p[j]));
    }
    SB();
    if (w == 0) poll_flags(ws, 4096 + cg * 128, 31, lane);
    wait_vm<0>();
#pragma unroll
    for (int i = 8; i < 14; ++i) wsp_read(i);
    lgkm0(); bar();
  }

  {
    static_for<0, 6>([&](auto U) {
      g2_issue(U, 0); g2_issue(U, 1);
      if constexpr (U.value == 1 || U.value == 3) {
        constexpr int j = 2 + (U.value >> 1);
        z1p[j] = u32x2{0u, 0u};
        if (j < ncnt) z1p[j] = gelu4(fin[j]);
        pin(z1p[j]);
      }
    });
#pragma unroll
    for (int q = 0; q < 4; ++q) wsp_issue(14 + (q >> 1), q & 1);
    wait_vm<12>();
    bar();
    f4 z1f[4];
    u32 po, po2;
#pragma unroll
    for (int j = 0; j < 7; ++j) fa[j] = frag(IC<0>{}, j);
    static_for<0, 32>([&](auto U) {
      constexpr int u = U.value;
      if constexpr (u & 1) {
        wait_vm<(u < 5 ? 10 : 6)>();
        bar();
      }
      if constexpr (u == 6) { wsp_read(14); wsp_read(15); }
      if constexpr (u >= 26 && u < 30) {
        constexpr int j = u - 26;
        z1f[j] = f4{bf_lo(z1p[j][0]), bf_hi(z1p[j][0]), bf_lo(z1p[j][1]), bf_hi(z1p[j][1])};
        pin4(z1f[j]);
      }
      if constexpr (u == 30) {
        po = (u32)WS_P + ((u32)(rt * 108 + 2 * (t0 + nfirst + (g & 1)) + (g >> 1)) * 64 + mt * 16 + n) * 16;
        po2 = 2 + (g & 1) < ncnt ? po + 4096 : 0x7ffffff0u;
        pin1(po); pin1(po2);
      }
#pragma unroll
      for (int j = 0; j < 7; ++j) {
        if (j == 0) wait_lgkm<3>();
        if (j == 4) wait_lgkm<(u + 1 < 32) ? 4 : 0>();
        acc[j] = mfma_b(fa[j], wf[u], u == 0 ? z : acc[j]);
        SB();
        if constexpr (u + 1 < 32) fa[j] = frag(IC<u + 1>{}, j);
        if (j == 1 || j == 4) {
          if constexpr (u + 6 < 32) g2_issue(IC<u + 6>{}, j == 4);
          else wo_issue(IC<u + 6 - 32>{}, j == 4);
        }
        SB();
      }
    });
    reduce_put();
    lgkm0(); bar();
    wo_issue(IC<6>{}, 0); wo_issue(IC<6>{}, 1); wo_issue(IC<7>{}, 0); wo_issue(IC<7>{}, 1);
    reduce_get();
    u32x2 o[4];
#pragma unroll
    for (int j = 0; j < 4; ++j) {
      u32 g0 = pk_bf(fin[j][0], fin[j][1]), g1 = pk_bf(fin[j][2], fin[j][3]);
      g0 = pk_bf(bf_lo(g0) + biasv, bf_hi(g0) + biasv);
      g1 = pk_bf(bf_lo(g1) + biasv, bf_hi(g1) + biasv);
      o[j] = u32x2{pk_bf(bf_lo(g0) * z1f[j][0], bf_hi(g0) * z1f[j][1]), pk_bf(bf_lo(g1) * z1f[j][2], bf_hi(g1) * z1f[j][3])};
    }
    st16_dev(wsr, po, widen(o[0], o[1]));
    st16_dev(wsr, po2, widen(o[2], o[3]));
  }

  {
    wait_vm<0>();
    lgkm0(); bar();
    if (tid == 0) set_flag(ws, 8192 + rt * 128 + cg * 4);
    const int dfirst = h ? 3 : 0;
    if (w == 0) poll_flags(ws, 8192 + rt * 128, 7, lane);
    else bdma0<127 * 1024, true>(srd, l16, (u32)WS_WOUT + cg * 138240 + (127 + w) * 1024, smb + w * 1024);
    bar();
    u32x4 pf[14];
    const u32 pvo = (u32)WS_P + ((u32)(rt * 108 + 52 * h + g) * 64 + mt * 16 + n) * 16;
    static_for<0, 6>([&](auto I) { ld16_async<0>(pf[I.value], srd, pvo, I.value * 4096); });
    u32x2 hv[3];
    const u32 ho = (u32)WS_H + hoff + (u32)(cg * 10 + 6 * h + (g >> 1)) * 128 + (g & 1) * 8;
    u32 wpa0 = smb + ((h * 13 * 4 + g) * 80 + n) * 16 + h * 768, wpb0 = wpa0 - h * 1280;
    u32 wpa1 = wpa0 + 7 * 5120, wpb1 = wpb0 + 7 * 5120;
    u32 oo = (u32)(srow + n) * 1280 + (cg * 5 + dfirst) * 32 + g * 8;
    asm("" : "+v"(wpa1), "+v"(wpb0), "+v"(wpb1), "+v"(oo));
    auto frag3 = [&](int i, int j) {
      return lds_rd((j < 2 ? (i < 7 ? wpa0 : wpa1) : (i < 7 ? wpb0 : wpb1)) + (i < 7 ? i : i - 7) * 5120 + j * 256);
    };
#pragma unroll
    for (int j = 0; j < 5; ++j) fa[j] = frag3(0, j);
    static_for<0, 14>([&](auto I) {
      constexpr int i = I.value;
      wait_vm_tie<(i < 9 ? 5 : 16 - i)>(pf[i]);
      if constexpr (i == 0) {
        if (h) pf[0] = u32x4{0u, 0u, 0u, 0u};
      }
      if constexpr (i == 8) bar();
#pragma unroll
      for (int j = 0; j < 5; ++j) {
        if (j == 0) wait_lgkm<2>();
        if (j == 3) wait_lgkm<(i + 1 < 14) ? 3 : 0>();
        acc[j] = mfma_b(fa[j], pf[i], i == 0 ? z : acc[j]);
        SB();
        if constexpr (i + 1 < 14)
          fa[j] = frag3(i + 1, j);
        if constexpr (i + 6 < 14) {
          if (j == 2) ld16_async<0>(pf[i + 6], srd, pvo, (i + 6) * 4096);
        } else if constexpr (i == 8) {
          if (j == 2) { ld8_async<0>(hv[0], srd, ho); ld8_async<256>(hv[1], srd, ho); ld8_async<512>(hv[2], srd, ho); }
        }
        SB();
      }
    });
    {
      f4* red = (f4*)(sm + G3_RED) + w * 192 + lane;
#pragma unroll
      for (int j = 0; j < 3; ++j) red[j * 64] = acc[2 + j];
    }
    asm volatile("s_waitcnt vmcnt(0)" : "+v"(hv[0]), "+v"(hv[1]), "+v"(hv[2])::"memory");
    lgkm0(); bar();
    {
      const f4* red = (const f4*)(sm + G3_RED) + (w ^ 4) * 192 + h * 64 + lane;
#pragma unroll
      for (int j = 0; j < 3; ++j) fin[j] = acc[j] + red[j * 64];
    }
#pragma unroll
    for (int j = 0; j < 3; ++j) {
      if (j < 2 || h == 0) {
        u32 o0 = pk_bf(fin[j][0], fin[j][1]), o1 = pk_bf(fin[j][2], fin[j][3]);
        u32x2 o = {pk_bf(bf_lo(hv[j][0]) + bf_lo(o0), bf_hi(hv[j][0]) + bf_hi(o0)),
                   pk_bf(bf_lo(hv[j][1]) + bf_lo(o1), bf_hi(hv[j][1]) + bf_hi(o1))};
        *(u32x2*)((char*)out + oo + j * 32) = o;
      }
    }
  }
}

extern "C" __global__ void k_text_end() {}

extern "C" void kernel_launch(void* const* d_in, const int* in_sizes, int n_in, void* d_out, int out_size,
                              void* d_ws, size_t ws_size, hipStream_t stream) {
  (void)in_sizes; (void)n_in; (void)out_size; (void)ws_size;
  char* ws = (char*)d_ws;
  (void)hipFuncSetAttribute((const void*)k2_mega, hipFuncAttributeMaxDynamicSharedMemorySize, MK_LDS);
  k1_fwd<<<512 + K1_NWIN + K1_NFILL, 512, K1_LDS, stream>>>((const u32*)d_in[0], (const u32x4*)d_in[1],
                                                            (const u32x4*)d_in[2], (const char*)d_in[4], ws);
  k2_mega<<<256, 512, MK_LDS, stream>>>((const char*)d_in[0], (const char*)d_in[5], (const char*)d_in[6], (const char*)d_in[7], ws,
                                        (u32x2*)d_out);
}
```
